```python
import torch

def _softmax(x, axis=-1):
    return torch.softmax(x, dim=axis)
def _gelu(x):
    return torch.nn.functional.gelu(x, approximate="tanh")
def _silu(x):
    return torch.nn.functional.silu(x)
def _sigmoid(x):
    return torch.sigmoid(x)
def _layernorm(x, axis=-1, eps=1e-5):
    m = x.mean(dim=axis, keepdim=True)
    v = x.var(dim=axis, keepdim=True, unbiased=False)
    return (x - m) / torch.sqrt(v + eps)
def _scatter_add(tgt, idx, src, axis=0):
    return tgt.clone().index_add_(axis, idx, src)
def _segment_sum(src, seg, n):
    out = torch.zeros((n,) + src.shape[1:], dtype=src.dtype)
    return out.index_add_(0, seg, src)
def _pad(x, pw):
    flat = tuple(v for pair in reversed(pw) for v in pair)
    return torch.nn.functional.pad(x, flat)

kernel_name = 'gated_mix__mha_block__linear_attn_block__prompt'

def setup_inputs():

    _w = (lambda a: (lambda t: t.to(torch.bfloat16) if t.is_floating_point() else t)(torch.as_tensor(a)))
    def _A_setup():
        B, S, D, H = 1, 2048, 576, 32
        return {
            "x": _w((lambda *s: torch.randn(*s, dtype=torch.bfloat16) * 0.05)(B, S, D)),
            "wqkv": _w((lambda *s: torch.randn(*s, dtype=torch.bfloat16) * 0.8)(D, 3 * D)),
            "wo": _w((lambda *s: torch.randn(*s, dtype=torch.bfloat16) * 0.05)(D, D)),
        }
    _A_in = _A_setup()

    def _B_setup():
        B, S, D = 1, 2048, 576
        return {
            "x": _w((lambda *s: torch.randn(*s, dtype=torch.bfloat16) * 0.05)(B, S, D)),
            "wqkv": _w((lambda *s: torch.randn(*s, dtype=torch.bfloat16) * 0.8)(D, 3 * D)),
            "wo": _w((lambda *s: torch.randn(*s, dtype=torch.bfloat16) * 0.1)(D, D)),
        }
    _B_in = _B_setup()

    return {
        "A_x": _A_in["x"],
        "A_wqkv": _A_in["wqkv"],
        "A_wo": _A_in["wo"],
        "B_x": _B_in["x"],
        "B_wqkv": _B_in["wqkv"],
        "B_wo": _B_in["wo"],
        "gate_w": _w((lambda *s: torch.randn(*s, dtype=torch.bfloat16) * 0.05)(576) * 16.0),
    }

def reference(A_x, A_wqkv, A_wo, B_x, B_wqkv, B_wo, gate_w):
    def _A_ref(x, wqkv, wo):
        B, S, D = x.shape
        H = 32
        Dh = D // H
        qkv = (lambda x, *d: x.permute(*d))((x @ wqkv).reshape(B, S, 3, H, Dh), 2, 0, 3, 1, 4)
        q, k, v = qkv[0], qkv[1], qkv[2]
        scores = torch.einsum("bhqd,bhkd->bhqk", q, k) * (Dh ** -0.5)
        p = _softmax(scores, axis=-1)
        o = torch.einsum("bhqk,bhkd->bhqd", p, v)
        return (lambda x, *d: x.permute(*d))(o, 0, 2, 1, 3).reshape(B, S, D) @ wo

    def _B_ref(x, wqkv, wo):
        B, S, D = x.shape
        H = 32; Dh = D // H
        qkv = (lambda x, *d: x.permute(*d))((x @ wqkv).reshape(B, S, 3, H, Dh), 2, 0, 3, 1, 4)
        q, k, v = qkv[0], qkv[1], qkv[2]
        phi = lambda t: torch.where(t > 0, t + 1, torch.exp(t)) + 1e-6
        pq, pk = phi(q), phi(k)
        kv = torch.einsum("bhsd,bhse->bhde", pk, v)
        z = torch.einsum("bhsd->bhd", pk)
        num = torch.einsum("bhqd,bhde->bhqe", pq, kv)
        den = torch.einsum("bhqd,bhd->bhq", pq, z)[..., None] + 1e-6
        o = num / den
        return (lambda x, *d: x.permute(*d))(o, 0, 2, 1, 3).reshape(B, S, D) @ wo

    x = A_x
    g = _sigmoid((x * gate_w).sum(-1, keepdims=True))
    a = _A_ref(x, A_wqkv, A_wo)
    b = _B_ref(x, B_wqkv, B_wo)
    return g * a + (1 - g) * b
```

```cpp
#include <hip/hip_runtime.h>
#include <hip/hip_bf16.h>
#include <stdint.h>

typedef __attribute__((ext_vector_type(8))) __bf16 bf16x8;
typedef __attribute__((ext_vector_type(2))) __bf16 bf16x2;
typedef __attribute__((ext_vector_type(4))) short s16x4;
typedef __attribute__((ext_vector_type(2))) float f32x2;
typedef __attribute__((ext_vector_type(4))) float f32x4;
typedef __attribute__((ext_vector_type(16))) float f32x16;
typedef __attribute__((ext_vector_type(32))) float f32x32;
typedef __attribute__((ext_vector_type(2))) unsigned u32x2;
typedef __attribute__((ext_vector_type(4))) unsigned u32x4;
typedef __attribute__((ext_vector_type(8))) unsigned u32x8;
typedef __attribute__((ext_vector_type(16))) unsigned u32x16;
#define DI __device__ __forceinline__
#define LAS __attribute__((address_space(3)))
typedef LAS char* ldsp;
typedef __amdgpu_buffer_rsrc_t rsrc_t;

constexpr int XROW = 1152;
constexpr int WROW = 3456;
constexpr int KV_GRP = 9728;
constexpr int KV_HEAD = 16 * KV_GRP;
constexpr int Q_GRP = 4608;
constexpr int Q_HEAD = 16 * Q_GRP;
constexpr int KVP_SZ = 1536;
constexpr int WS_KV = 0;
constexpr int WS_Q = WS_KV + 32 * KV_HEAD;
constexpr int WS_PQ = WS_Q + 32 * Q_HEAD;
constexpr int WS_OA = WS_Q;
constexpr int WS_OB = WS_PQ;
constexpr int WS_KVP = WS_PQ + 32 * Q_HEAD;
constexpr int WS_G = WS_KVP + 32 * 16 * KVP_SZ;
constexpr int WS_FLG = WS_G + 2 * 2048 * 4;
constexpr int WS_END = WS_FLG + 320 * 1024;
constexpr float QSCALE = 0.23570226039551584f * 1.4426950408889634f;
constexpr float LOG2E = 1.4426950408889634f;
constexpr int LDS_BYTES = 160 * 1024;
constexpr int AUX = 16;

struct Params { const char* x; const char* wa; const char* woa; const char* wb; const char* wob; const char* gw; char* out; char* ws; };

DI unsigned pk2(float a, float b) { f32x2 v = {a, b}; return __builtin_bit_cast(unsigned, __builtin_convertvector(v, bf16x2)); }
DI u32x4 pk8(f32x4 a, f32x4 b) { return (u32x4){pk2(a[0], a[1]), pk2(a[2], a[3]), pk2(b[0], b[1]), pk2(b[2], b[3])}; }
DI u32x4 pk8s(f32x4 a, f32x4 b, bool sw) { const u32x4 v = pk8(a, b); return sw ? (u32x4){v[2], v[3], v[0], v[1]} : v; }
DI f32x16 mfma32(u32x4 a, u32x4 b, f32x16 c) { return __builtin_amdgcn_mfma_f32_32x32x16_bf16(__builtin_bit_cast(bf16x8, a), __builtin_bit_cast(bf16x8, b), c, 0, 0, 0); }
DI u32x4 lds128(ldsp p) { return *(LAS u32x4*)p; }
DI void st128(rsrc_t r, int off, u32x4 v) { __builtin_amdgcn_raw_buffer_store_b128(v, r, off, 0, AUX); }
DI void st128s(rsrc_t r, int off, int so, u32x4 v) {
  __builtin_amdgcn_raw_buffer_store_b128(v, r, off, so, AUX); asm volatile("s_nop 1" :: "v"(v) : "memory"); }
DI void st32(rsrc_t r, int off, unsigned v) { __builtin_amdgcn_raw_buffer_store_b32(v, r, off, 0, AUX); }
DI u32x4 ld128(rsrc_t r, int off, int so = 0) { return __builtin_amdgcn_raw_buffer_load_b128(r, off, so, AUX); }
DI unsigned ld32(rsrc_t r, int off, int so = 0) { return __builtin_amdgcn_raw_buffer_load_b32(r, off, so, AUX); }
DI unsigned phi2(float a, float b) {
  const f32x2 t = {a, b}, l = t * LOG2E, p = t + 1e-6f;
  const f32x2 r = (f32x2){fmaxf(p[0], 1e-6f), fmaxf(p[1], 1e-6f)} +
                  (f32x2){__builtin_amdgcn_fmed3f(__builtin_amdgcn_exp2f(l[0]), 0.f, 1.f), __builtin_amdgcn_fmed3f(__builtin_amdgcn_exp2f(l[1]), 0.f, 1.f)};
  return pk2(r[0], r[1]); }
DI u32x2 phi4(f32x4 v) { return (u32x2){phi2(v[0], v[1]), phi2(v[2], v[3])}; }
DI u32x2 pk4(f32x4 v) { return (u32x2){pk2(v[0], v[1]), pk2(v[2], v[3])}; }
DI void dma(const char* base, unsigned voff, unsigned lds) {
  asm volatile("s_mov_b32 m0, %2\n\ts_nop 0\n\tglobal_load_lds_dwordx4 %0, %1" :: "v"(voff), "s"(base), "s"(lds) : "memory"); }
DI void dma_sc1(const char* base, unsigned voff, unsigned lds) {
  asm volatile("s_mov_b32 m0, %2\n\ts_nop 0\n\tglobal_load_lds_dwordx4 %0, %1 sc1" :: "v"(voff), "s"(base), "s"(lds) : "memory"); }
DI void dma32(const char* base, unsigned voff, unsigned lds) {
  asm volatile("s_mov_b32 m0, %2\n\ts_mov_b32 exec_hi, 0\n\tglobal_load_lds_dwordx4 %0, %1\n\ts_mov_b32 exec_hi, -1" :: "v"(voff), "s"(base), "s"(lds) : "memory"); }
DI void touch(const char* base, unsigned voff, unsigned lds) {
  asm volatile("s_mov_b32 m0, %2\n\ts_nop 0\n\tglobal_load_lds_dword %0, %1" :: "v"(voff), "s"(base), "s"(lds) : "memory"); }
DI void touch_sc1(const char* base, unsigned voff, unsigned lds) {
  asm volatile("s_mov_b32 m0, %2\n\ts_nop 0\n\tglobal_load_lds_dword %0, %1 sc1" :: "v"(voff), "s"(base), "s"(lds) : "memory"); }
template <int N> DI void wait_bar() { asm volatile("s_waitcnt vmcnt(%0) lgkmcnt(0)\n\ts_barrier" :: "i"(N) : "memory"); }
DI void lds_bar() { asm volatile("s_waitcnt lgkmcnt(0)\n\ts_barrier" ::: "memory"); }

#define ATT_PRO \
  "v_mov_b32 v105, 0x100000\n\t" \
  "ds_read_b128 v[56:59], v105\n\t" \
  "ds_read_b128 v[60:63], v105\n\t" \
  "ds_read_b128 v[64:67], v105\n\t" \
  "ds_read_b128 v[68:71], v105\n\t" \
  "ds_read_b128 v[72:75], v105\n\t" \
  "ds_read_b128 v[76:79], v105\n\t" \
  "ds_read_b128 v[80:83], v105\n\t" \
  "ds_read_b128 v[84:87], v105\n\t" \
  "ds_read_b128 v[88:91], v105\n\t" \
  "ds_read_b128 v[92:95], v105\n\t" \
  "ds_read_b128 v[132:135], v105\n\t" \
  "v_mov_b32 v105, 0\n\t" \
  "v_mov_b32 v106, 0\n\t" \
  "v_add_u32 v152, 0x80, v110\n\t" \
  "v_mov_b32 v156, v146\n\t" \
  "s_cmp_lt_u32 %[f], 128\n\t" \
  "s_waitcnt vmcnt(3) lgkmcnt(0)\n\t" \
  "s_cbranch_scc1 .Lpro%=\n\t" \
  "s_waitcnt vmcnt(0)\n\t" \
  ".Lpro%=:\n\t" \
  "s_barrier\n\t" \
  "ds_read_b32 v104, %[qr]\n\t" \
  "ds_read2st64_b32 v[136:137], v110 offset0:0 offset1:1\n\t" \
  "ds_read_b128 v[96:99], %[qa]\n\t" \
  "ds_read_b128 v[112:115], v109\n\t" \
  "ds_read_b128 v[100:103], %[qa] offset:1024\n\t" \
  "ds_read_b128 v[116:119], v109 offset:1024\n\t" \
  "ds_read_b128 v[120:123], v111\n\t" \
  "ds_read_b128 v[124:127], v111 offset:640\n\t" \
  "ds_read2st64_b32 v[138:139], v152 offset0:0 offset1:1\n\t" \
  "s_waitcnt lgkmcnt(7)\n\t" \
  "v_mfma_f32_32x32x4_2b_bf16 v[0:31], v[136:137], v[104:105], 0\n\t" \
  "v_mov_b32 v107, v104\n\t" \
  "s_nop 15\n\t" \
  "s_waitcnt lgkmcnt(0)\n\t" \
  "v_mfma_f32_32x32x16_bf16 v[0:15], v[112:115], v[96:99], v[0:15]\n\t" \
  "v_mfma_f32_32x32x16_bf16 v[16:31], v[112:115], v[100:103], v[16:31]\n\t" \
  "s_nop 10\n\t"
#define ATT_LOOP \
  ".Latt%=:\n\t" \
  "v_exp_f32 v0, v0\n\t" \
  "v_exp_f32 v1, v1\n\t" \
  "v_smfmac_f32_32x32x32_bf16 v[80:95], v[132:135], v[56:63], v108\n\t" \
  "s_waitcnt lgkmcnt(0)\n\t" \
  "ds_read_b128 v[132:135], v111 offset:1920\n\t" \
  "v_exp_f32 v2, v2\n\t" \
  "v_exp_f32 v3, v3\n\t" \
  "v_exp_f32 v4, v4\n\t" \
  "v_mfma_f32_32x32x4_2b_bf16 v[32:63], v[138:139], v[104:105], 0\n\t" \
  "v_exp_f32 v5, v5\n\t" \
  "v_exp_f32 v6, v6\n\t" \
  "v_exp_f32 v7, v7\n\t" \
  "v_exp_f32 v16, v16\n\t" \
  "v_exp_f32 v17, v17\n\t" \
  "v_exp_f32 v18, v18\n\t" \
  "v_exp_f32 v19, v19\n\t" \
  "v_exp_f32 v20, v20\n\t" \
  "v_smfmac_f32_32x32x32_bf16 v[64:79], v[120:123], v[0:7], v108\n\t" \
  "ds_read_b128 v[128:131], v111 offset:1280\n\t" \
  "v_exp_f32 v21, v21\n\t" \
  "v_exp_f32 v22, v22\n\t" \
  "v_exp_f32 v23, v23\n\t" \
  "v_exp_f32 v8, v8\n\t" \
  "v_exp_f32 v9, v9\n\t" \
  "v_smfmac_f32_32x32x32_bf16 v[80:95], v[120:123], v[16:23], v108\n\t" \
  "ds_read_b128 v[112:115], v109 offset:2048\n\t" \
  "v_exp_f32 v10, v10\n\t" \
  "v_exp_f32 v11, v11\n\t" \
  "v_exp_f32 v12, v12\n\t" \
  "v_exp_f32 v13, v13\n\t" \
  "v_mfma_f32_32x32x16_bf16 v[32:47], v[116:119], v[96:99], v[32:47]\n\t" \
  "v_exp_f32 v14, v14\n\t" \
  "v_exp_f32 v15, v15\n\t" \
  "v_exp_f32 v24, v24\n\t" \
  "v_exp_f32 v25, v25\n\t" \
  "v_mfma_f32_32x32x16_bf16 v[48:63], v[116:119], v[100:103], v[48:63]\n\t" \
  "v_exp_f32 v26, v26\n\t" \
  "v_exp_f32 v27, v27\n\t" \
  "v_exp_f32 v28, v28\n\t" \
  "v_exp_f32 v29, v29\n\t" \
  "v_smfmac_f32_32x32x32_bf16 v[64:79], v[124:127], v[8:15], v108\n\t" \
  "v_exp_f32 v30, v30\n\t" \
  "v_exp_f32 v31, v31\n\t" \
  "v_exp_f32 v32, v32\n\t" \
  "v_exp_f32 v33, v33\n\t" \
  "v_smfmac_f32_32x32x32_bf16 v[80:95], v[124:127], v[24:31], v108\n\t" \
  "s_waitcnt lgkmcnt(0)\n\t" \
  "ds_read_b128 v[124:127], v111 offset:3200\n\t" \
  "v_exp_f32 v34, v34\n\t" \
  "v_exp_f32 v35, v35\n\t" \
  "v_exp_f32 v36, v36\n\t" \
  "v_mfma_f32_32x32x4_2b_bf16 v[0:31], v[136:137], v[106:107], 0\n\t" \
  "v_exp_f32 v37, v37\n\t" \
  "v_exp_f32 v38, v38\n\t" \
  "v_exp_f32 v39, v39\n\t" \
  "v_exp_f32 v48, v48\n\t" \
  "v_exp_f32 v49, v49\n\t" \
  "v_exp_f32 v50, v50\n\t" \
  "v_exp_f32 v51, v51\n\t" \
  "v_exp_f32 v52, v52\n\t" \
  "v_smfmac_f32_32x32x32_bf16 v[64:79], v[128:131], v[32:39], v108\n\t" \
  "ds_read_b128 v[120:123], v111 offset:2560\n\t" \
  "v_exp_f32 v53, v53\n\t" \
  "v_exp_f32 v54, v54\n\t" \
  "v_exp_f32 v55, v55\n\t" \
  "v_exp_f32 v40, v40\n\t" \
  "v_exp_f32 v41, v41\n\t" \
  "v_smfmac_f32_32x32x32_bf16 v[80:95], v[128:131], v[48:55], v108\n\t" \
  "ds_read_b128 v[116:119], v109 offset:3072\n\t" \
  "v_exp_f32 v42, v42\n\t" \
  "v_exp_f32 v43, v43\n\t" \
  "v_exp_f32 v44, v44\n\t" \
  "v_exp_f32 v45, v45\n\t" \
  "v_mfma_f32_32x32x16_bf16 v[0:15], v[112:115], v[96:99], v[0:15]\n\t" \
  "ds_read2st64_b32 v[142:143], v152 offset0:38 offset1:39\n\t" \
  "v_exp_f32 v46, v46\n\t" \
  "v_exp_f32 v47, v47\n\t" \
  "v_exp_f32 v56, v56\n\t" \
  "v_exp_f32 v57, v57\n\t" \
  "v_mfma_f32_32x32x16_bf16 v[16:31], v[112:115], v[100:103], v[16:31]\n\t" \
  "v_exp_f32 v58, v58\n\t" \
  "v_exp_f32 v59, v59\n\t" \
  "v_exp_f32 v60, v60\n\t" \
  "v_exp_f32 v61, v61\n\t" \
  "v_smfmac_f32_32x32x32_bf16 v[64:79], v[132:135], v[40:47], v108\n\t" \
  "v_exp_f32 v62, v62\n\t" \
  "v_exp_f32 v63, v63\n\t" \
  "v_exp_f32 v0, v0\n\t" \
  "v_exp_f32 v1, v1\n\t" \
  "v_smfmac_f32_32x32x32_bf16 v[80:95], v[132:135], v[56:63], v108\n\t" \
  "s_waitcnt lgkmcnt(0)\n\t" \
  "ds_read_b128 v[132:135], v111 offset:4480\n\t" \
  "v_exp_f32 v2, v2\n\t" \
  "v_exp_f32 v3, v3\n\t" \
  "v_exp_f32 v4, v4\n\t" \
  "v_mfma_f32_32x32x4_2b_bf16 v[32:63], v[138:139], v[106:107], 0\n\t" \
  "v_exp_f32 v5, v5\n\t" \
  "v_exp_f32 v6, v6\n\t" \
  "v_exp_f32 v7, v7\n\t" \
  "v_exp_f32 v16, v16\n\t" \
  "v_exp_f32 v17, v17\n\t" \
  "v_exp_f32 v18, v18\n\t" \
  "v_exp_f32 v19, v19\n\t" \
  "v_exp_f32 v20, v20\n\t" \
  "v_smfmac_f32_32x32x32_bf16 v[64:79], v[120:123], v[0:7], v108\n\t" \
  "ds_read_b128 v[128:131], v111 offset:3840\n\t" \
  "v_exp_f32 v21, v21\n\t" \
  "v_exp_f32 v22, v22\n\t" \
  "v_exp_f32 v23, v23\n\t" \
  "v_exp_f32 v8, v8\n\t" \
  "v_exp_f32 v9, v9\n\t" \
  "v_smfmac_f32_32x32x32_bf16 v[80:95], v[120:123], v[16:23], v108\n\t" \
  "ds_read_b128 v[112:115], v109 offset:9728\n\t" \
  "v_exp_f32 v10, v10\n\t" \
  "v_exp_f32 v11, v11\n\t" \
  "v_exp_f32 v12, v12\n\t" \
  "v_exp_f32 v13, v13\n\t" \
  "v_mfma_f32_32x32x16_bf16 v[32:47], v[116:119], v[96:99], v[32:47]\n\t" \
  "ds_read2st64_b32 v[140:141], v110 offset0:38 offset1:39\n\t" \
  "v_exp_f32 v14, v14\n\t" \
  "v_exp_f32 v15, v15\n\t" \
  "v_exp_f32 v24, v24\n\t" \
  "v_exp_f32 v25, v25\n\t" \
  "v_mfma_f32_32x32x16_bf16 v[48:63], v[116:119], v[100:103], v[48:63]\n\t" \
  "v_exp_f32 v26, v26\n\t" \
  "v_exp_f32 v27, v27\n\t" \
  "v_exp_f32 v28, v28\n\t" \
  "v_exp_f32 v29, v29\n\t" \
  "v_smfmac_f32_32x32x32_bf16 v[64:79], v[124:127], v[8:15], v108\n\t" \
  "v_exp_f32 v30, v30\n\t" \
  "v_exp_f32 v31, v31\n\t" \
  "v_exp_f32 v32, v32\n\t" \
  "v_exp_f32 v33, v33\n\t" \
  "v_smfmac_f32_32x32x32_bf16 v[80:95], v[124:127], v[24:31], v108\n\t" \
  "s_waitcnt vmcnt(0) lgkmcnt(0)\n\t" \
  "ds_read_b128 v[124:127], v111 offset:10368\n\t" \
  "v_exp_f32 v34, v34\n\t" \
  "v_exp_f32 v35, v35\n\t" \
  "v_exp_f32 v36, v36\n\t" \
  "v_mfma_f32_32x32x4_2b_bf16 v[0:31], v[140:141], v[104:105], 0\n\t" \
  "v_exp_f32 v37, v37\n\t" \
  "v_exp_f32 v38, v38\n\t" \
  "v_exp_f32 v39, v39\n\t" \
  "v_exp_f32 v48, v48\n\t" \
  "v_exp_f32 v49, v49\n\t" \
  "v_exp_f32 v50, v50\n\t" \
  "v_exp_f32 v51, v51\n\t" \
  "v_exp_f32 v52, v52\n\t" \
  "v_smfmac_f32_32x32x32_bf16 v[64:79], v[128:131], v[32:39], v108\n\t" \
  "ds_read_b128 v[120:123], v111 offset:9728\n\t" \
  "v_exp_f32 v53, v53\n\t" \
  "v_exp_f32 v54, v54\n\t" \
  "v_exp_f32 v55, v55\n\t" \
  "v_exp_f32 v40, v40\n\t" \
  "v_exp_f32 v41, v41\n\t" \
  "v_smfmac_f32_32x32x32_bf16 v[80:95], v[128:131], v[48:55], v108\n\t" \
  "ds_read_b128 v[116:119], v109 offset:10752\n\t" \
  "v_exp_f32 v42, v42\n\t" \
  "v_exp_f32 v43, v43\n\t" \
  "v_exp_f32 v44, v44\n\t" \
  "v_exp_f32 v45, v45\n\t" \
  "v_mfma_f32_32x32x16_bf16 v[0:15], v[112:115], v[96:99], v[0:15]\n\t" \
  "ds_add_u32 v144, v145 offset:0\n\t" \
  "v_exp_f32 v46, v46\n\t" \
  "v_exp_f32 v47, v47\n\t" \
  "v_exp_f32 v56, v56\n\t" \
  "v_exp_f32 v57, v57\n\t" \
  "v_mfma_f32_32x32x16_bf16 v[16:31], v[112:115], v[100:103], v[16:31]\n\t" \
  "v_exp_f32 v58, v58\n\t" \
  "v_exp_f32 v59, v59\n\t" \
  "v_exp_f32 v60, v60\n\t" \
  "v_exp_f32 v61, v61\n\t" \
  "v_smfmac_f32_32x32x32_bf16 v[64:79], v[132:135], v[40:47], v108\n\t" \
  "v_exp_f32 v62, v62\n\t" \
  "v_exp_f32 v63, v63\n\t" \
  "v_exp_f32 v0, v0\n\t" \
  "v_exp_f32 v1, v1\n\t" \
  "v_smfmac_f32_32x32x32_bf16 v[80:95], v[132:135], v[56:63], v108\n\t" \
  "s_waitcnt lgkmcnt(0)\n\t" \
  "ds_read_b128 v[132:135], v111 offset:11648\n\t" \
  "v_exp_f32 v2, v2\n\t" \
  "v_exp_f32 v3, v3\n\t" \
  "v_exp_f32 v4, v4\n\t" \
  "v_mfma_f32_32x32x4_2b_bf16 v[32:63], v[142:143], v[104:105], 0\n\t" \
  "v_exp_f32 v5, v5\n\t" \
  "v_exp_f32 v6, v6\n\t" \
  "v_exp_f32 v7, v7\n\t" \
  "v_exp_f32 v16, v16\n\t" \
  "v_exp_f32 v17, v17\n\t" \
  "v_exp_f32 v18, v18\n\t" \
  "v_exp_f32 v19, v19\n\t" \
  "v_exp_f32 v20, v20\n\t" \
  "v_smfmac_f32_32x32x32_bf16 v[64:79], v[120:123], v[0:7], v108\n\t" \
  "ds_read_b128 v[128:131], v111 offset:11008\n\t" \
  "v_exp_f32 v21, v21\n\t" \
  "v_exp_f32 v22, v22\n\t" \
  "v_exp_f32 v23, v23\n\t" \
  "v_exp_f32 v8, v8\n\t" \
  "v_exp_f32 v9, v9\n\t" \
  "v_smfmac_f32_32x32x32_bf16 v[80:95], v[120:123], v[16:23], v108\n\t" \
  "ds_read_b128 v[112:115], v109 offset:11776\n\t" \
  "v_exp_f32 v10, v10\n\t" \
  "v_exp_f32 v11, v11\n\t" \
  "v_exp_f32 v12, v12\n\t" \
  "v_exp_f32 v13, v13\n\t" \
  "v_mfma_f32_32x32x16_bf16 v[32:47], v[116:119], v[96:99], v[32:47]\n\t" \
  "v_exp_f32 v14, v14\n\t" \
  "v_exp_f32 v15, v15\n\t" \
  "v_exp_f32 v24, v24\n\t" \
  "v_exp_f32 v25, v25\n\t" \
  "v_mfma_f32_32x32x16_bf16 v[48:63], v[116:119], v[100:103], v[48:63]\n\t" \
  "v_exp_f32 v26, v26\n\t" \
  "v_exp_f32 v27, v27\n\t" \
  "v_exp_f32 v28, v28\n\t" \
  "v_exp_f32 v29, v29\n\t" \
  "v_smfmac_f32_32x32x32_bf16 v[64:79], v[124:127], v[8:15], v108\n\t" \
  "v_exp_f32 v30, v30\n\t" \
  "v_exp_f32 v31, v31\n\t" \
  "v_exp_f32 v32, v32\n\t" \
  "v_exp_f32 v33, v33\n\t" \
  "v_smfmac_f32_32x32x32_bf16 v[80:95], v[124:127], v[24:31], v108\n\t" \
  "s_waitcnt lgkmcnt(0)\n\t" \
  "ds_read_b128 v[124:127], v111 offset:12928\n\t" \
  "v_exp_f32 v34, v34\n\t" \
  "v_exp_f32 v35, v35\n\t" \
  "v_exp_f32 v36, v36\n\t" \
  "v_mfma_f32_32x32x4_2b_bf16 v[0:31], v[140:141], v[106:107], 0\n\t" \
  "v_exp_f32 v37, v37\n\t" \
  "v_exp_f32 v38, v38\n\t" \
  "v_exp_f32 v39, v39\n\t" \
  "v_exp_f32 v48, v48\n\t" \
  "v_exp_f32 v49, v49\n\t" \
  "v_exp_f32 v50, v50\n\t" \
  "v_exp_f32 v51, v51\n\t" \
  "v_exp_f32 v52, v52\n\t" \
  "v_smfmac_f32_32x32x32_bf16 v[64:79], v[128:131], v[32:39], v108\n\t" \
  "ds_read_b128 v[120:123], v111 offset:12288\n\t" \
  "v_exp_f32 v53, v53\n\t" \
  "v_exp_f32 v54, v54\n\t" \
  "v_exp_f32 v55, v55\n\t" \
  "v_exp_f32 v40, v40\n\t" \
  "v_exp_f32 v41, v41\n\t" \
  "v_smfmac_f32_32x32x32_bf16 v[80:95], v[128:131], v[48:55], v108\n\t" \
  "ds_read_b128 v[116:119], v109 offset:12800\n\t" \
  "v_exp_f32 v42, v42\n\t" \
  "v_exp_f32 v43, v43\n\t" \
  "v_exp_f32 v44, v44\n\t" \
  "v_exp_f32 v45, v45\n\t" \
  "v_mfma_f32_32x32x16_bf16 v[0:15], v[112:115], v[96:99], v[0:15]\n\t" \
  "ds_read2st64_b32 v[138:139], v152 offset0:76 offset1:77\n\t" \
  "v_exp_f32 v46, v46\n\t" \
  "v_exp_f32 v47, v47\n\t" \
  "v_exp_f32 v56, v56\n\t" \
  "v_exp_f32 v57, v57\n\t" \
  "v_mfma_f32_32x32x16_bf16 v[16:31], v[112:115], v[100:103], v[16:31]\n\t" \
  "v_exp_f32 v58, v58\n\t" \
  "v_exp_f32 v59, v59\n\t" \
  "v_exp_f32 v60, v60\n\t" \
  "v_exp_f32 v61, v61\n\t" \
  "v_smfmac_f32_32x32x32_bf16 v[64:79], v[132:135], v[40:47], v108\n\t" \
  "v_exp_f32 v62, v62\n\t" \
  "v_exp_f32 v63, v63\n\t" \
  "v_exp_f32 v0, v0\n\t" \
  "v_exp_f32 v1, v1\n\t" \
  "v_smfmac_f32_32x32x32_bf16 v[80:95], v[132:135], v[56:63], v108\n\t" \
  "s_waitcnt lgkmcnt(0)\n\t" \
  "ds_read_b128 v[132:135], v111 offset:14208\n\t" \
  "v_exp_f32 v2, v2\n\t" \
  "v_exp_f32 v3, v3\n\t" \
  "v_exp_f32 v4, v4\n\t" \
  "v_mfma_f32_32x32x4_2b_bf16 v[32:63], v[142:143], v[106:107], 0\n\t" \
  "v_exp_f32 v5, v5\n\t" \
  "v_exp_f32 v6, v6\n\t" \
  "v_exp_f32 v7, v7\n\t" \
  "v_exp_f32 v16, v16\n\t" \
  "v_exp_f32 v17, v17\n\t" \
  "v_exp_f32 v18, v18\n\t" \
  "v_exp_f32 v19, v19\n\t" \
  "v_exp_f32 v20, v20\n\t" \
  "v_smfmac_f32_32x32x32_bf16 v[64:79], v[120:123], v[0:7], v108\n\t" \
  "ds_read_b128 v[128:131], v111 offset:13568\n\t" \
  "v_exp_f32 v21, v21\n\t" \
  "v_exp_f32 v22, v22\n\t" \
  "v_exp_f32 v23, v23\n\t" \
  "v_exp_f32 v8, v8\n\t" \
  "v_exp_f32 v9, v9\n\t" \
  "v_smfmac_f32_32x32x32_bf16 v[80:95], v[120:123], v[16:23], v108\n\t" \
  "ds_read_b128 v[112:115], v109 offset:19456\n\t" \
  "v_exp_f32 v10, v10\n\t" \
  "v_exp_f32 v11, v11\n\t" \
  "v_exp_f32 v12, v12\n\t" \
  "v_exp_f32 v13, v13\n\t" \
  "v_mfma_f32_32x32x16_bf16 v[32:47], v[116:119], v[96:99], v[32:47]\n\t" \
  "ds_read2st64_b32 v[136:137], v110 offset0:76 offset1:77\n\t" \
  "v_exp_f32 v14, v14\n\t" \
  "v_exp_f32 v15, v15\n\t" \
  "v_exp_f32 v24, v24\n\t" \
  "v_exp_f32 v25, v25\n\t" \
  "v_mfma_f32_32x32x16_bf16 v[48:63], v[116:119], v[100:103], v[48:63]\n\t" \
  "v_exp_f32 v26, v26\n\t" \
  "v_exp_f32 v27, v27\n\t" \
  "v_exp_f32 v28, v28\n\t" \
  "v_exp_f32 v29, v29\n\t" \
  "v_smfmac_f32_32x32x32_bf16 v[64:79], v[124:127], v[8:15], v108\n\t" \
  "v_exp_f32 v30, v30\n\t" \
  "v_exp_f32 v31, v31\n\t" \
  "v_exp_f32 v32, v32\n\t" \
  "v_exp_f32 v33, v33\n\t" \
  "v_smfmac_f32_32x32x32_bf16 v[80:95], v[124:127], v[24:31], v108\n\t" \
  "s_waitcnt lgkmcnt(0)\n\t" \
  "ds_read_b128 v[124:127], v111 offset:20096\n\t" \
  "v_exp_f32 v34, v34\n\t" \
  "v_exp_f32 v35, v35\n\t" \
  "v_exp_f32 v36, v36\n\t" \
  "v_mfma_f32_32x32x4_2b_bf16 v[0:31], v[136:137], v[104:105], 0\n\t" \
  "v_exp_f32 v37, v37\n\t" \
  "v_exp_f32 v38, v38\n\t" \
  "v_exp_f32 v39, v39\n\t" \
  "v_exp_f32 v48, v48\n\t" \
  "v_exp_f32 v49, v49\n\t" \
  "v_exp_f32 v50, v50\n\t" \
  "v_exp_f32 v51, v51\n\t" \
  "v_exp_f32 v52, v52\n\t" \
  "v_smfmac_f32_32x32x32_bf16 v[64:79], v[128:131], v[32:39], v108\n\t" \
  "ds_read_b128 v[120:123], v111 offset:19456\n\t" \
  "v_exp_f32 v53, v53\n\t" \
  "v_exp_f32 v54, v54\n\t" \
  "v_exp_f32 v55, v55\n\t" \
  "v_exp_f32 v40, v40\n\t" \
  "v_exp_f32 v41, v41\n\t" \
  "v_smfmac_f32_32x32x32_bf16 v[80:95], v[128:131], v[48:55], v108\n\t" \
  "ds_read_b128 v[116:119], v109 offset:20480\n\t" \
  "v_exp_f32 v42, v42\n\t" \
  "v_exp_f32 v43, v43\n\t" \
  "v_exp_f32 v44, v44\n\t" \
  "v_exp_f32 v45, v45\n\t" \
  "v_mfma_f32_32x32x16_bf16 v[0:15], v[112:115], v[96:99], v[0:15]\n\t" \
  "ds_add_u32 v144, v145 offset:4\n\t" \
  "v_exp_f32 v46, v46\n\t" \
  "v_exp_f32 v47, v47\n\t" \
  "v_exp_f32 v56, v56\n\t" \
  "v_exp_f32 v57, v57\n\t" \
  "v_mfma_f32_32x32x16_bf16 v[16:31], v[112:115], v[100:103], v[16:31]\n\t" \
  "v_exp_f32 v58, v58\n\t" \
  "v_exp_f32 v59, v59\n\t" \
  "v_exp_f32 v60, v60\n\t" \
  "v_exp_f32 v61, v61\n\t" \
  "v_smfmac_f32_32x32x32_bf16 v[64:79], v[132:135], v[40:47], v108\n\t" \
  "v_exp_f32 v62, v62\n\t" \
  "v_exp_f32 v63, v63\n\t" \
  "v_exp_f32 v0, v0\n\t" \
  "v_exp_f32 v1, v1\n\t" \
  "v_smfmac_f32_32x32x32_bf16 v[80:95], v[132:135], v[56:63], v108\n\t" \
  "s_waitcnt lgkmcnt(0)\n\t" \
  "ds_read_b128 v[132:135], v111 offset:21376\n\t" \
  "v_exp_f32 v2, v2\n\t" \
  "v_exp_f32 v3, v3\n\t" \
  "v_exp_f32 v4, v4\n\t" \
  "v_mfma_f32_32x32x4_2b_bf16 v[32:63], v[138:139], v[104:105], 0\n\t" \
  "v_exp_f32 v5, v5\n\t" \
  "v_exp_f32 v6, v6\n\t" \
  "v_exp_f32 v7, v7\n\t" \
  "v_exp_f32 v16, v16\n\t" \
  "v_exp_f32 v17, v17\n\t" \
  "v_exp_f32 v18, v18\n\t" \
  "v_exp_f32 v19, v19\n\t" \
  "v_exp_f32 v20, v20\n\t" \
  "v_smfmac_f32_32x32x32_bf16 v[64:79], v[120:123], v[0:7], v108\n\t" \
  "ds_read_b128 v[128:131], v111 offset:20736\n\t" \
  "v_exp_f32 v21, v21\n\t" \
  "v_exp_f32 v22, v22\n\t" \
  "v_exp_f32 v23, v23\n\t" \
  "v_exp_f32 v8, v8\n\t" \
  "v_exp_f32 v9, v9\n\t" \
  "v_smfmac_f32_32x32x32_bf16 v[80:95], v[120:123], v[16:23], v108\n\t" \
  "ds_read_b128 v[112:115], v109 offset:21504\n\t" \
  "v_exp_f32 v10, v10\n\t" \
  "v_exp_f32 v11, v11\n\t" \
  "v_exp_f32 v12, v12\n\t" \
  "v_exp_f32 v13, v13\n\t" \
  "v_mfma_f32_32x32x16_bf16 v[32:47], v[116:119], v[96:99], v[32:47]\n\t" \
  "v_exp_f32 v14, v14\n\t" \
  "v_exp_f32 v15, v15\n\t" \
  "v_exp_f32 v24, v24\n\t" \
  "v_exp_f32 v25, v25\n\t" \
  "v_mfma_f32_32x32x16_bf16 v[48:63], v[116:119], v[100:103], v[48:63]\n\t" \
  "v_exp_f32 v26, v26\n\t" \
  "v_exp_f32 v27, v27\n\t" \
  "v_exp_f32 v28, v28\n\t" \
  "v_exp_f32 v29, v29\n\t" \
  "v_smfmac_f32_32x32x32_bf16 v[64:79], v[124:127], v[8:15], v108\n\t" \
  "v_exp_f32 v30, v30\n\t" \
  "v_exp_f32 v31, v31\n\t" \
  "v_exp_f32 v32, v32\n\t" \
  "v_exp_f32 v33, v33\n\t" \
  "v_smfmac_f32_32x32x32_bf16 v[80:95], v[124:127], v[24:31], v108\n\t" \
  "s_waitcnt lgkmcnt(0)\n\t" \
  "ds_read_b128 v[124:127], v111 offset:22656\n\t" \
  "v_exp_f32 v34, v34\n\t" \
  "v_exp_f32 v35, v35\n\t" \
  "v_exp_f32 v36, v36\n\t" \
  "v_mfma_f32_32x32x4_2b_bf16 v[0:31], v[136:137], v[106:107], 0\n\t" \
  "v_exp_f32 v37, v37\n\t" \
  "v_exp_f32 v38, v38\n\t" \
  "v_exp_f32 v39, v39\n\t" \
  "v_exp_f32 v48, v48\n\t" \
  "v_exp_f32 v49, v49\n\t" \
  "v_exp_f32 v50, v50\n\t" \
  "v_exp_f32 v51, v51\n\t" \
  "v_exp_f32 v52, v52\n\t" \
  "v_smfmac_f32_32x32x32_bf16 v[64:79], v[128:131], v[32:39], v108\n\t" \
  "ds_read_b128 v[120:123], v111 offset:22016\n\t" \
  "v_exp_f32 v53, v53\n\t" \
  "v_exp_f32 v54, v54\n\t" \
  "v_exp_f32 v55, v55\n\t" \
  "v_exp_f32 v40, v40\n\t" \
  "v_exp_f32 v41, v41\n\t" \
  "v_smfmac_f32_32x32x32_bf16 v[80:95], v[128:131], v[48:55], v108\n\t" \
  "ds_read_b128 v[116:119], v109 offset:22528\n\t" \
  "v_exp_f32 v42, v42\n\t" \
  "v_exp_f32 v43, v43\n\t" \
  "v_exp_f32 v44, v44\n\t" \
  "v_exp_f32 v45, v45\n\t" \
  "v_mfma_f32_32x32x16_bf16 v[0:15], v[112:115], v[96:99], v[0:15]\n\t" \
  "ds_read2st64_b32 v[142:143], v152 offset0:114 offset1:115\n\t" \
  "v_exp_f32 v46, v46\n\t" \
  "v_exp_f32 v47, v47\n\t" \
  "v_exp_f32 v56, v56\n\t" \
  "v_exp_f32 v57, v57\n\t" \
  "v_mfma_f32_32x32x16_bf16 v[16:31], v[112:115], v[100:103], v[16:31]\n\t" \
  "v_exp_f32 v58, v58\n\t" \
  "v_exp_f32 v59, v59\n\t" \
  "v_exp_f32 v60, v60\n\t" \
  "v_exp_f32 v61, v61\n\t" \
  "v_smfmac_f32_32x32x32_bf16 v[64:79], v[132:135], v[40:47], v108\n\t" \
  "v_exp_f32 v62, v62\n\t" \
  "v_exp_f32 v63, v63\n\t" \
  "v_exp_f32 v0, v0\n\t" \
  "v_exp_f32 v1, v1\n\t" \
  "v_smfmac_f32_32x32x32_bf16 v[80:95], v[132:135], v[56:63], v108\n\t" \
  "s_waitcnt lgkmcnt(0)\n\t" \
  "ds_read_b128 v[132:135], v111 offset:23936\n\t" \
  "v_exp_f32 v2, v2\n\t" \
  "v_exp_f32 v3, v3\n\t" \
  "v_exp_f32 v4, v4\n\t" \
  "v_mfma_f32_32x32x4_2b_bf16 v[32:63], v[138:139], v[106:107], 0\n\t" \
  "v_exp_f32 v5, v5\n\t" \
  "v_exp_f32 v6, v6\n\t" \
  "v_exp_f32 v7, v7\n\t" \
  "v_exp_f32 v16, v16\n\t" \
  "v_exp_f32 v17, v17\n\t" \
  "v_exp_f32 v18, v18\n\t" \
  "v_exp_f32 v19, v19\n\t" \
  "v_exp_f32 v20, v20\n\t" \
  "v_smfmac_f32_32x32x32_bf16 v[64:79], v[120:123], v[0:7], v108\n\t" \
  "ds_read_b128 v[128:131], v111 offset:23296\n\t" \
  "v_exp_f32 v21, v21\n\t" \
  "v_exp_f32 v22, v22\n\t" \
  "v_exp_f32 v23, v23\n\t" \
  "v_exp_f32 v8, v8\n\t" \
  "v_exp_f32 v9, v9\n\t" \
  "v_smfmac_f32_32x32x32_bf16 v[80:95], v[120:123], v[16:23], v108\n\t" \
  "ds_read_b128 v[112:115], v109 offset:29184\n\t" \
  "v_exp_f32 v10, v10\n\t" \
  "v_exp_f32 v11, v11\n\t" \
  "v_exp_f32 v12, v12\n\t" \
  "v_exp_f32 v13, v13\n\t" \
  "v_mfma_f32_32x32x16_bf16 v[32:47], v[116:119], v[96:99], v[32:47]\n\t" \
  "ds_read2st64_b32 v[140:141], v110 offset0:114 offset1:115\n\t" \
  "v_exp_f32 v14, v14\n\t" \
  "v_exp_f32 v15, v15\n\t" \
  "v_exp_f32 v24, v24\n\t" \
  "v_exp_f32 v25, v25\n\t" \
  "v_mfma_f32_32x32x16_bf16 v[48:63], v[116:119], v[100:103], v[48:63]\n\t" \
  "v_exp_f32 v26, v26\n\t" \
  "v_exp_f32 v27, v27\n\t" \
  "v_exp_f32 v28, v28\n\t" \
  "v_exp_f32 v29, v29\n\t" \
  "v_smfmac_f32_32x32x32_bf16 v[64:79], v[124:127], v[8:15], v108\n\t" \
  "v_exp_f32 v30, v30\n\t" \
  "v_exp_f32 v31, v31\n\t" \
  "v_exp_f32 v32, v32\n\t" \
  "v_exp_f32 v33, v33\n\t" \
  "v_smfmac_f32_32x32x32_bf16 v[80:95], v[124:127], v[24:31], v108\n\t" \
  "s_waitcnt lgkmcnt(0)\n\t" \
  "ds_read_b128 v[124:127], v111 offset:29824\n\t" \
  "v_exp_f32 v34, v34\n\t" \
  "v_exp_f32 v35, v35\n\t" \
  "v_exp_f32 v36, v36\n\t" \
  "v_mfma_f32_32x32x4_2b_bf16 v[0:31], v[140:141], v[104:105], 0\n\t" \
  "v_exp_f32 v37, v37\n\t" \
  "v_exp_f32 v38, v38\n\t" \
  "v_exp_f32 v39, v39\n\t" \
  "v_exp_f32 v48, v48\n\t" \
  "v_exp_f32 v49, v49\n\t" \
  "v_exp_f32 v50, v50\n\t" \
  "v_exp_f32 v51, v51\n\t" \
  "v_exp_f32 v52, v52\n\t" \
  "v_smfmac_f32_32x32x32_bf16 v[64:79], v[128:131], v[32:39], v108\n\t" \
  "ds_read_b128 v[120:123], v111 offset:29184\n\t" \
  "v_exp_f32 v53, v53\n\t" \
  "v_exp_f32 v54, v54\n\t" \
  "v_exp_f32 v55, v55\n\t" \
  "v_exp_f32 v40, v40\n\t" \
  "v_exp_f32 v41, v41\n\t" \
  "v_smfmac_f32_32x32x32_bf16 v[80:95], v[128:131], v[48:55], v108\n\t" \
  "ds_read_b128 v[116:119], v109 offset:30208\n\t" \
  "v_exp_f32 v42, v42\n\t" \
  "v_exp_f32 v43, v43\n\t" \
  "v_exp_f32 v44, v44\n\t" \
  "v_exp_f32 v45, v45\n\t" \
  "v_mfma_f32_32x32x16_bf16 v[0:15], v[112:115], v[96:99], v[0:15]\n\t" \
  "ds_add_u32 v144, v145 offset:8\n\t" \
  "v_exp_f32 v46, v46\n\t" \
  "v_exp_f32 v47, v47\n\t" \
  "v_exp_f32 v56, v56\n\t" \
  "v_exp_f32 v57, v57\n\t" \
  "v_mfma_f32_32x32x16_bf16 v[16:31], v[112:115], v[100:103], v[16:31]\n\t" \
  "v_exp_f32 v58, v58\n\t" \
  "v_exp_f32 v59, v59\n\t" \
  "v_exp_f32 v60, v60\n\t" \
  "v_exp_f32 v61, v61\n\t" \
  "v_smfmac_f32_32x32x32_bf16 v[64:79], v[132:135], v[40:47], v108\n\t" \
  "v_exp_f32 v62, v62\n\t" \
  "v_exp_f32 v63, v63\n\t" \
  "v_exp_f32 v0, v0\n\t" \
  "v_exp_f32 v1, v1\n\t" \
  "v_smfmac_f32_32x32x32_bf16 v[80:95], v[132:135], v[56:63], v108\n\t" \
  "s_waitcnt lgkmcnt(0)\n\t" \
  "ds_read_b128 v[132:135], v111 offset:31104\n\t" \
  "v_exp_f32 v2, v2\n\t" \
  "v_exp_f32 v3, v3\n\t" \
  "v_exp_f32 v4, v4\n\t" \
  "v_mfma_f32_32x32x4_2b_bf16 v[32:63], v[142:143], v[104:105], 0\n\t" \
  "v_exp_f32 v5, v5\n\t" \
  "v_exp_f32 v6, v6\n\t" \
  "v_exp_f32 v7, v7\n\t" \
  "v_exp_f32 v16, v16\n\t" \
  "v_exp_f32 v17, v17\n\t" \
  "v_exp_f32 v18, v18\n\t" \
  "v_exp_f32 v19, v19\n\t" \
  "v_exp_f32 v20, v20\n\t" \
  "v_smfmac_f32_32x32x32_bf16 v[64:79], v[120:123], v[0:7], v108\n\t" \
  "ds_read_b128 v[128:131], v111 offset:30464\n\t" \
  "v_exp_f32 v21, v21\n\t" \
  "v_exp_f32 v22, v22\n\t" \
  "v_exp_f32 v23, v23\n\t" \
  "v_exp_f32 v8, v8\n\t" \
  "v_exp_f32 v9, v9\n\t" \
  "v_smfmac_f32_32x32x32_bf16 v[80:95], v[120:123], v[16:23], v108\n\t" \
  "ds_read_b128 v[112:115], v109 offset:31232\n\t" \
  "v_exp_f32 v10, v10\n\t" \
  "v_exp_f32 v11, v11\n\t" \
  "v_exp_f32 v12, v12\n\t" \
  "v_exp_f32 v13, v13\n\t" \
  "v_mfma_f32_32x32x16_bf16 v[32:47], v[116:119], v[96:99], v[32:47]\n\t" \
  "v_exp_f32 v14, v14\n\t" \
  "v_exp_f32 v15, v15\n\t" \
  "v_exp_f32 v24, v24\n\t" \
  "v_exp_f32 v25, v25\n\t" \
  "v_mfma_f32_32x32x16_bf16 v[48:63], v[116:119], v[100:103], v[48:63]\n\t" \
  "v_exp_f32 v26, v26\n\t" \
  "v_exp_f32 v27, v27\n\t" \
  "v_exp_f32 v28, v28\n\t" \
  "v_exp_f32 v29, v29\n\t" \
  "v_smfmac_f32_32x32x32_bf16 v[64:79], v[124:127], v[8:15], v108\n\t" \
  "v_exp_f32 v30, v30\n\t" \
  "v_exp_f32 v31, v31\n\t" \
  "v_exp_f32 v32, v32\n\t" \
  "v_exp_f32 v33, v33\n\t" \
  "v_smfmac_f32_32x32x32_bf16 v[80:95], v[124:127], v[24:31], v108\n\t" \
  "s_waitcnt lgkmcnt(0)\n\t" \
  "ds_read_b128 v[124:127], v111 offset:32384\n\t" \
  "v_exp_f32 v34, v34\n\t" \
  "v_exp_f32 v35, v35\n\t" \
  "v_exp_f32 v36, v36\n\t" \
  "v_mfma_f32_32x32x4_2b_bf16 v[0:31], v[140:141], v[106:107], 0\n\t" \
  "v_exp_f32 v37, v37\n\t" \
  "v_exp_f32 v38, v38\n\t" \
  "v_exp_f32 v39, v39\n\t" \
  "v_exp_f32 v48, v48\n\t" \
  "v_exp_f32 v49, v49\n\t" \
  "v_exp_f32 v50, v50\n\t" \
  "v_exp_f32 v51, v51\n\t" \
  "v_exp_f32 v52, v52\n\t" \
  "v_smfmac_f32_32x32x32_bf16 v[64:79], v[128:131], v[32:39], v108\n\t" \
  "ds_read_b128 v[120:123], v111 offset:31744\n\t" \
  "v_exp_f32 v53, v53\n\t" \
  "v_exp_f32 v54, v54\n\t" \
  "v_exp_f32 v55, v55\n\t" \
  "v_exp_f32 v40, v40\n\t" \
  "v_exp_f32 v41, v41\n\t" \
  "v_smfmac_f32_32x32x32_bf16 v[80:95], v[128:131], v[48:55], v108\n\t" \
  "ds_read_b128 v[116:119], v109 offset:32256\n\t" \
  "v_exp_f32 v42, v42\n\t" \
  "v_exp_f32 v43, v43\n\t" \
  "v_exp_f32 v44, v44\n\t" \
  "v_exp_f32 v45, v45\n\t" \
  "v_mfma_f32_32x32x16_bf16 v[0:15], v[112:115], v[96:99], v[0:15]\n\t" \
  "ds_read2st64_b32 v[138:139], v152 offset0:152 offset1:153\n\t" \
  "v_exp_f32 v46, v46\n\t" \
  "v_exp_f32 v47, v47\n\t" \
  "v_exp_f32 v56, v56\n\t" \
  "v_exp_f32 v57, v57\n\t" \
  "v_mfma_f32_32x32x16_bf16 v[16:31], v[112:115], v[100:103], v[16:31]\n\t" \
  "v_exp_f32 v58, v58\n\t" \
  "v_exp_f32 v59, v59\n\t" \
  "v_exp_f32 v60, v60\n\t" \
  "v_exp_f32 v61, v61\n\t" \
  "v_smfmac_f32_32x32x32_bf16 v[64:79], v[132:135], v[40:47], v108\n\t" \
  "v_exp_f32 v62, v62\n\t" \
  "v_exp_f32 v63, v63\n\t" \
  "v_exp_f32 v0, v0\n\t" \
  "v_exp_f32 v1, v1\n\t" \
  "v_smfmac_f32_32x32x32_bf16 v[80:95], v[132:135], v[56:63], v108\n\t" \
  "s_waitcnt lgkmcnt(0)\n\t" \
  "ds_read_b128 v[132:135], v111 offset:33664\n\t" \
  "v_exp_f32 v2, v2\n\t" \
  "v_exp_f32 v3, v3\n\t" \
  "v_exp_f32 v4, v4\n\t" \
  "v_mfma_f32_32x32x4_2b_bf16 v[32:63], v[142:143], v[106:107], 0\n\t" \
  "v_exp_f32 v5, v5\n\t" \
  "v_exp_f32 v6, v6\n\t" \
  "v_exp_f32 v7, v7\n\t" \
  "v_exp_f32 v16, v16\n\t" \
  "v_exp_f32 v17, v17\n\t" \
  "v_exp_f32 v18, v18\n\t" \
  "v_exp_f32 v19, v19\n\t" \
  "v_exp_f32 v20, v20\n\t" \
  "v_smfmac_f32_32x32x32_bf16 v[64:79], v[120:123], v[0:7], v108\n\t" \
  "ds_read_b128 v[128:131], v111 offset:33024\n\t" \
  "v_exp_f32 v21, v21\n\t" \
  "v_exp_f32 v22, v22\n\t" \
  "v_exp_f32 v23, v23\n\t" \
  "v_exp_f32 v8, v8\n\t" \
  "v_exp_f32 v9, v9\n\t" \
  "v_smfmac_f32_32x32x32_bf16 v[80:95], v[120:123], v[16:23], v108\n\t" \
  "ds_read_b128 v[112:115], v109 offset:38912\n\t" \
  "v_exp_f32 v10, v10\n\t" \
  "v_exp_f32 v11, v11\n\t" \
  "v_exp_f32 v12, v12\n\t" \
  "v_exp_f32 v13, v13\n\t" \
  "v_mfma_f32_32x32x16_bf16 v[32:47], v[116:119], v[96:99], v[32:47]\n\t" \
  "ds_read2st64_b32 v[136:137], v110 offset0:152 offset1:153\n\t" \
  "v_exp_f32 v14, v14\n\t" \
  "v_exp_f32 v15, v15\n\t" \
  "v_exp_f32 v24, v24\n\t" \
  "v_exp_f32 v25, v25\n\t" \
  "v_mfma_f32_32x32x16_bf16 v[48:63], v[116:119], v[100:103], v[48:63]\n\t" \
  "ds_read_b32 v153, v156\n\t" \
  "v_exp_f32 v26, v26\n\t" \
  "v_exp_f32 v27, v27\n\t" \
  "v_exp_f32 v28, v28\n\t" \
  "v_exp_f32 v29, v29\n\t" \
  "v_smfmac_f32_32x32x32_bf16 v[64:79], v[124:127], v[8:15], v108\n\t" \
  "v_exp_f32 v30, v30\n\t" \
  "v_exp_f32 v31, v31\n\t" \
  "v_exp_f32 v32, v32\n\t" \
  "v_exp_f32 v33, v33\n\t" \
  "v_smfmac_f32_32x32x32_bf16 v[80:95], v[124:127], v[24:31], v108\n\t" \
  "s_waitcnt lgkmcnt(0)\n\t" \
  "ds_read_b128 v[124:127], v111 offset:39552\n\t" \
  "v_exp_f32 v34, v34\n\t" \
  "v_exp_f32 v35, v35\n\t" \
  "v_exp_f32 v36, v36\n\t" \
  "v_mfma_f32_32x32x4_2b_bf16 v[0:31], v[136:137], v[104:105], 0\n\t" \
  "v_readfirstlane_b32 %[t], v153\n\t" \
  "v_exp_f32 v37, v37\n\t" \
  "v_exp_f32 v38, v38\n\t" \
  "v_exp_f32 v39, v39\n\t" \
  "v_exp_f32 v48, v48\n\t" \
  "v_exp_f32 v49, v49\n\t" \
  "v_exp_f32 v50, v50\n\t" \
  "v_exp_f32 v51, v51\n\t" \
  "v_exp_f32 v52, v52\n\t" \
  "v_smfmac_f32_32x32x32_bf16 v[64:79], v[128:131], v[32:39], v108\n\t" \
  "s_cmp_lt_u32 %[t], 4\n\t" \
  "s_cbranch_scc1 .Lattw%=\n\t" \
  ".Lattc%=:\n\t" \
  "ds_read_b128 v[120:123], v111 offset:38912\n\t" \
  "v_exp_f32 v53, v53\n\t" \
  "v_exp_f32 v54, v54\n\t" \
  "v_exp_f32 v55, v55\n\t" \
  "v_exp_f32 v40, v40\n\t" \
  "v_exp_f32 v41, v41\n\t" \
  "v_smfmac_f32_32x32x32_bf16 v[80:95], v[128:131], v[48:55], v108\n\t" \
  "ds_read_b128 v[116:119], v109 offset:39936\n\t" \
  "s_sub_u32 %[n], %[n], 1\n\t" \
  "v_exp_f32 v42, v42\n\t" \
  "v_exp_f32 v43, v43\n\t" \
  "v_exp_f32 v44, v44\n\t" \
  "v_exp_f32 v45, v45\n\t" \
  "v_mfma_f32_32x32x16_bf16 v[0:15], v[112:115], v[96:99], v[0:15]\n\t" \
  "ds_add_u32 v144, v145 offset:12\n\t" \
  "s_cmp_lg_u32 %[n], 0\n\t" \
  "v_exp_f32 v46, v46\n\t" \
  "v_exp_f32 v47, v47\n\t" \
  "v_exp_f32 v56, v56\n\t" \
  "v_exp_f32 v57, v57\n\t" \
  "v_mfma_f32_32x32x16_bf16 v[16:31], v[112:115], v[100:103], v[16:31]\n\t" \
  "ds_read2_b32 v[148:149], v146 offset0:4 offset1:7\n\t" \
  "ds_read2_b32 v[150:151], v147 offset0:0 offset1:32\n\t" \
  "v_add_u32 v109, %[inc], v109\n\t" \
  "v_add_u32 v110, %[inc], v110\n\t" \
  "v_add_u32 v111, %[inc], v111\n\t" \
  "v_add_u32 v152, %[inc], v152\n\t" \
  "v_add_u32 v144, 16, v144\n\t" \
  "v_add_u32 v156, 100, v146\n\t" \
  "v_exp_f32 v58, v58\n\t" \
  "v_exp_f32 v59, v59\n\t" \
  "v_exp_f32 v60, v60\n\t" \
  "v_exp_f32 v61, v61\n\t" \
  "v_smfmac_f32_32x32x32_bf16 v[64:79], v[132:135], v[40:47], v108\n\t" \
  "v_exp_f32 v62, v62\n\t" \
  "v_exp_f32 v63, v63\n\t" \
  "s_cbranch_scc1 .Latt%=\n\t" \
  "s_branch .Latte%=\n\t" \
  ".Lattw%=:\n\t" \
  "s_mov_b32 %[sp], 0x400000\n\t" \
  ".Lattx%=:\n\t" \
  "s_sleep 2\n\t" \
  "ds_read_b32 v153, v156\n\t" \
  "s_waitcnt lgkmcnt(0)\n\t" \
  "v_readfirstlane_b32 %[t], v153\n\t" \
  "s_sub_u32 %[sp], %[sp], 1\n\t" \
  "s_cbranch_scc1 .Lattc%=\n\t" \
  "s_cmp_lt_u32 %[t], 4\n\t" \
  "s_cbranch_scc1 .Lattx%=\n\t" \
  "s_branch .Lattc%=\n\t" \
  ".Lattg%=:\n\t" \
  "s_mov_b32 %[sp], 0x400000\n\t" \
  ".Latty%=:\n\t" \
  "s_sleep 2\n\t" \
  "ds_read2_b32 v[148:149], v146 offset0:4 offset1:7\n\t" \
  "ds_read2_b32 v[150:151], v147 offset0:0 offset1:32\n\t" \
  "s_waitcnt lgkmcnt(0)\n\t" \
  "v_readfirstlane_b32 %[t], v148\n\t" \
  "s_sub_u32 %[sp], %[sp], 1\n\t" \
  "s_cbranch_scc1 .Latth%=\n\t" \
  "s_cmp_lt_u32 %[t], 4\n\t" \
  "s_cbranch_scc1 .Latty%=\n\t" \
  "s_branch .Latth%=\n\t" \
  ".Latte%=:\n\t" \
  "v_smfmac_f32_32x32x32_bf16 v[80:95], v[132:135], v[56:63], v108\n\t" \
  "s_waitcnt lgkmcnt(0)\n\t" \
  "v_readfirstlane_b32 %[t], v148\n\t" \
  "s_mov_b32 %[bad], 3\n\t" \
  "s_nop 1\n\t" \
  "s_cmp_lt_u32 %[t], 4\n\t" \
  "s_cbranch_scc1 .Lattg%=\n\t" \
  ".Latth%=:\n\t" \
  "v_mov_b32 v0, v74\n\t" \
  "v_maximum3_f32 v1, |v64|, |v65|, |v66|\n\t" \
  "v_maximum3_f32 v2, |v67|, |v68|, |v69|\n\t" \
  "v_permlane32_swap_b32 v74, v0\n\t" \
  "v_maximum3_f32 v1, v1, |v70|, |v71|\n\t" \
  "v_rcp_f32 v3, v74\n\t" \
  "v_maximum3_f32 v2, v2, |v72|, |v73|\n\t" \
  "v_cmp_nle_f32 vcc, %[lo], v74\n\t" \
  "v_mul_f32 v3, v3, v150\n\t" \
  "v_maximum3_f32 v1, v1, v2, v74\n\t" \
  "v_mul_f32 v4, v3, v64\n\t" \
  "v_mul_f32 v5, v3, v65\n\t" \
  "v_cmp_nge_f32 %[m], %[hi], v1\n\t" \
  "v_mul_f32 v6, v3, v66\n\t" \
  "v_mul_f32 v7, v3, v67\n\t" \
  "v_mul_f32 v8, v3, v68\n\t" \
  "v_mul_f32 v9, v3, v69\n\t" \
  "v_mul_f32 v10, v3, v70\n\t" \
  "v_mul_f32 v11, v3, v71\n\t" \
  "v_mul_f32 v12, v3, v72\n\t" \
  "v_mul_f32 v13, v3, v73\n\t" \
  "v_cvt_pk_bf16_f32 v16, v4, v5\n\t" \
  "v_cvt_pk_bf16_f32 v17, v6, v7\n\t" \
  "v_cvt_pk_bf16_f32 v18, v8, v9\n\t" \
  "v_cvt_pk_bf16_f32 v19, v10, v11\n\t" \
  "v_cvt_pk_bf16_f32 v20, v12, v13\n\t" \
  "s_or_b64 vcc, vcc, %[m]\n\t" \
  "s_cbranch_scc1 .Lfn0_%=\n\t" \
  "buffer_store_dwordx4 v[16:19], v154, %[ws], %[om] offen offset:0 sc1\n\t" \
  "buffer_store_dword v20, v155, %[ws], %[orr] offen offset:0 sc1\n\t" \
  "s_bitset0_b32 %[bad], 0\n\t" \
  ".Lfn0_%=:\n\t" \
  "v_mov_b32 v0, v90\n\t" \
  "v_maximum3_f32 v1, |v80|, |v81|, |v82|\n\t" \
  "v_maximum3_f32 v2, |v83|, |v84|, |v85|\n\t" \
  "v_permlane32_swap_b32 v90, v0\n\t" \
  "v_maximum3_f32 v1, v1, |v86|, |v87|\n\t" \
  "v_rcp_f32 v3, v90\n\t" \
  "v_maximum3_f32 v2, v2, |v88|, |v89|\n\t" \
  "v_cmp_nle_f32 vcc, %[lo], v90\n\t" \
  "v_mul_f32 v3, v3, v151\n\t" \
  "v_maximum3_f32 v1, v1, v2, v90\n\t" \
  "v_mul_f32 v4, v3, v80\n\t" \
  "v_mul_f32 v5, v3, v81\n\t" \
  "v_cmp_nge_f32 %[m], %[hi], v1\n\t" \
  "v_mul_f32 v6, v3, v82\n\t" \
  "v_mul_f32 v7, v3, v83\n\t" \
  "v_mul_f32 v8, v3, v84\n\t" \
  "v_mul_f32 v9, v3, v85\n\t" \
  "v_mul_f32 v10, v3, v86\n\t" \
  "v_mul_f32 v11, v3, v87\n\t" \
  "v_mul_f32 v12, v3, v88\n\t" \
  "v_mul_f32 v13, v3, v89\n\t" \
  "v_cvt_pk_bf16_f32 v22, v4, v5\n\t" \
  "v_cvt_pk_bf16_f32 v23, v6, v7\n\t" \
  "v_cvt_pk_bf16_f32 v24, v8, v9\n\t" \
  "v_cvt_pk_bf16_f32 v25, v10, v11\n\t" \
  "v_cvt_pk_bf16_f32 v26, v12, v13\n\t" \
  "s_or_b64 vcc, vcc, %[m]\n\t" \
  "s_cbranch_scc1 .Lfn1_%=\n\t" \
  "buffer_store_dwordx4 v[22:25], v154, %[ws], %[om] offen offset:1024 sc1\n\t" \
  "buffer_store_dword v26, v155, %[ws], %[orr] offen offset:128 sc1\n\t" \
  "s_bitset0_b32 %[bad], 1\n\t" \
  ".Lfn1_%=:\n\t"

#define P1P0 \
  "s_cmp_lt_u32 %[tt], 8\n\t" \
  "s_cbranch_scc1 .Lp0t_%=\n\t" \
  "s_add_u32 m0, %[m1], 122880\n\t" \
  "s_nop 0\n\t" \
  "buffer_load_dwordx4 v237, %[ra], %[sw] offen lds\n\t" \
  "s_cmp_lg_u32 %[m1], %[l0]\n\t" \
  "s_cbranch_scc1 .Lp1f1_%=\n\t" \
  "s_add_u32 m0, %[l0], 131072\n\t" \
  "s_nop 0\n\t" \
  "buffer_load_dwordx4 v238, %[ra], %[sw] offen lds\n\t" \
  ".Lp1f1_%=:\n\t" \
  "s_barrier\n\t" \
  "s_add_u32 %[sx], %[sx], 128\n\t" \
  "s_cmp_eq_u32 %[sx], 0x480\n\t" \
  "s_cselect_b32 %[sx], 0, %[sx]\n\t" \
  "s_mul_i32 %[sw], %[sx], 0x6c0\n\t" \
  "s_add_u32 m0, %[m2], 16384\n\t" \
  "s_nop 0\n\t" \
  "buffer_load_dwordx4 v232, %[rx], %[sx] offen lds\n\t" \
  "s_add_u32 m0, %[m1], 132096\n\t" \
  "s_nop 0\n\t" \
  "buffer_load_dwordx4 v237, %[ra], %[sw] offen lds\n\t" \
  "s_cmp_lg_u32 %[m1], %[l0]\n\t" \
  "s_cbranch_scc1 .Lp1f2_%=\n\t" \
  "s_add_u32 m0, %[l0], 140288\n\t" \
  "s_nop 0\n\t" \
  "buffer_load_dwordx4 v238, %[ra], %[sw] offen lds\n\t" \
  ".Lp1f2_%=:\n\t" \
  "s_add_u32 m0, %[m2], 17408\n\t" \
  "s_nop 0\n\t" \
  "buffer_load_dwordx4 v233, %[rx], %[sx] offen lds\n\t" \
  "s_branch .Lp0e_%=\n\t" \
  ".Lp0t_%=:\n\t" \
  "s_add_u32 m0, %[m2], 86016\n\t" \
  "s_nop 0\n\t" \
  "buffer_load_dwordx4 v234, %[ra], %[sw] offen lds\n\t" \
  "s_cmp_lg_u32 %[e2], %[l0]\n\t" \
  "s_cbranch_scc1 .Lp1t1_%=\n\t" \
  "s_add_u32 m0, %[m0q], 102400\n\t" \
  "s_nop 0\n\t" \
  "buffer_load_dwordx4 v236, %[ra], %[sw] offen lds\n\t" \
  ".Lp1t1_%=:\n\t" \
  "s_add_u32 m0, %[m2], 87040\n\t" \
  "s_nop 0\n\t" \
  "buffer_load_dwordx4 v235, %[ra], %[sw] offen lds\n\t" \
  "s_barrier\n\t" \
  "s_add_u32 %[sx], %[sx], 128\n\t" \
  "s_cmp_eq_u32 %[sx], 0x480\n\t" \
  "s_cselect_b32 %[sx], 0, %[sx]\n\t" \
  "s_mul_i32 %[sw], %[sx], 0x6c0\n\t" \
  "s_add_u32 m0, %[m2], 16384\n\t" \
  "s_nop 0\n\t" \
  "buffer_load_dwordx4 v232, %[rx], %[sx] offen lds\n\t" \
  "s_add_u32 m0, %[m2], 104448\n\t" \
  "s_nop 0\n\t" \
  "buffer_load_dwordx4 v234, %[ra], %[sw] offen lds\n\t" \
  "s_cmp_lg_u32 %[e2], %[l0]\n\t" \
  "s_cbranch_scc1 .Lp1t2_%=\n\t" \
  "s_add_u32 m0, %[m0q], 120832\n\t" \
  "s_nop 0\n\t" \
  "buffer_load_dwordx4 v236, %[ra], %[sw] offen lds\n\t" \
  ".Lp1t2_%=:\n\t" \
  "s_add_u32 m0, %[m2], 17408\n\t" \
  "s_nop 0\n\t" \
  "buffer_load_dwordx4 v233, %[rx], %[sx] offen lds\n\t" \
  "s_add_u32 m0, %[m2], 105472\n\t" \
  "s_nop 0\n\t" \
  "buffer_load_dwordx4 v235, %[ra], %[sw] offen lds\n\t" \
  ".Lp0e_%=:\n\t"

#define P1FETCH \
  "s_add_u32 m0, %[m1], 150528\n\t" \
  "s_add_u32 %[sw], %[sq], 0\n\t" \
  "buffer_load_dwordx4 %[l16], %[rw], %[sw] offen sc1 lds\n\t" \
  "s_mov_b32 m0, %[m7]\n\t" \
  "s_nop 0\n\t" \
  "buffer_load_dwordx4 %[l16], %[rw], %[s7] offen sc1 lds\n\t" \
  "s_add_u32 m0, %[m1], 0\n\t" \
  "s_add_u32 %[sw], %[sk], 0\n\t" \
  "buffer_load_dwordx4 %[l16], %[rw], %[sw] offen sc1 lds\n\t" \
  "s_add_u32 m0, %[m1], 7168\n\t" \
  "s_add_u32 %[sw], %[sk], 7168\n\t" \
  "buffer_load_dwordx4 %[l16], %[rw], %[sw] offen sc1 lds\n\t" \
  "s_add_u32 m0, %[m1], 14336\n\t" \
  "s_add_u32 %[sw], %[sk], 14336\n\t" \
  "buffer_load_dwordx4 %[l16], %[rw], %[sw] offen sc1 lds\n\t" \
  "s_add_u32 m0, %[m1], 21504\n\t" \
  "s_add_u32 %[sw], %[sk], 21504\n\t" \
  "buffer_load_dwordx4 %[l16], %[rw], %[sw] offen sc1 lds\n\t" \
  "s_add_u32 m0, %[m1], 28672\n\t" \
  "s_add_u32 %[sw], %[sk], 28672\n\t" \
  "buffer_load_dwordx4 %[l16], %[rw], %[sw] offen sc1 lds\n\t" \
  "s_add_u32 m0, %[m1], 35840\n\t" \
  "s_add_u32 %[sw], %[sk], 35840\n\t" \
  "buffer_load_dwordx4 %[l16], %[rw], %[sw] offen sc1 lds\n\t"

#define P1A_T \
  "s_cmp_eq_u32 %[ch], 0\n\t" \
  "s_cbranch_scc1 .Lp1t3_%=\n\t" \
  "buffer_load_dwordx4 v[104:107], %[g1], %[rx], 0 offen offset:0\n\t" \
  "buffer_load_dwordx4 v[108:111], %[g0], %[rgw], 0 offen offset:0\n\t" \
  "buffer_load_dwordx4 v[112:115], %[g1], %[rx], 0 offen offset:256\n\t" \
  "buffer_load_dwordx4 v[116:119], %[g0], %[rgw], 0 offen offset:256\n\t" \
  "buffer_load_dwordx4 v[120:123], %[g1], %[rx], 0 offen offset:512\n\t" \
  "buffer_load_dwordx4 v[124:127], %[g0], %[rgw], 0 offen offset:512\n\t" \
  "buffer_load_dwordx4 v[128:131], %[g1], %[rx], 0 offen offset:768\n\t" \
  "buffer_load_dwordx4 v[132:135], %[g0], %[rgw], 0 offen offset:768\n\t" \
  "buffer_load_dwordx4 v[136:139], %[g2], %[rx], 0 offen offset:1024\n\t" \
  "buffer_load_dwordx4 v[140:143], %[g0], %[rgw], 0 offen offset:1024\n\t" \
  ".Lp1t3_%=:\n\t" \
  "s_cmp_lg_u32 %[ch], 0\n\t" \
  "s_waitcnt vmcnt(14) lgkmcnt(0)\n\t" \
  "s_cbranch_scc1 .Lp1t4_%=\n\t" \
  "s_waitcnt vmcnt(4)\n\t" \
  ".Lp1t4_%=:\n\t" \
  "s_barrier\n\t" \
  "ds_read_b128 v[0:3], v220 offset:0\n\t" \
  "ds_read_b128 v[4:7], v221 offset:0\n\t" \
  "ds_read_b64_tr_b16 v[180:181], v224 offset:36864\n\t" \
  "ds_read_b64_tr_b16 v[182:183], v224 offset:41472\n\t" \
  "ds_read_b64_tr_b16 v[184:185], v224 offset:36872\n\t" \
  "ds_read_b64_tr_b16 v[186:187], v224 offset:41480\n\t" \
  "ds_read_b64_tr_b16 v[188:189], v224 offset:36936\n\t" \
  "ds_read_b64_tr_b16 v[190:191], v224 offset:41544\n\t" \
  "ds_read_b64_tr_b16 v[192:193], v224 offset:36944\n\t" \
  "ds_read_b64_tr_b16 v[194:195], v224 offset:41552\n\t" \
  "ds_read_b64_tr_b16 v[196:197], v226 offset:36864\n\t" \
  "ds_read_b64_tr_b16 v[198:199], v226 offset:41472\n\t" \
  "s_waitcnt lgkmcnt(8)\n\t" \
  "v_mfma_f32_16x16x32_bf16 v[144:147], v[180:183], v[0:3], 0\n\t" \
  "ds_read_b128 v[8:11], v222 offset:0\n\t" \
  "v_mfma_f32_16x16x32_bf16 v[148:151], v[180:183], v[4:7], 0\n\t" \
  "s_add_u32 %[sx], %[sx], 128\n\t" \
  "s_cmp_eq_u32 %[sx], 0x480\n\t" \
  "s_cselect_b32 %[sx], 0, %[sx]\n\t" \
  "s_mul_i32 %[sw], %[sx], 0x6c0\n\t" \
  "s_add_u32 m0, %[m2], 32768\n\t" \
  "s_nop 0\n\t" \
  "buffer_load_dwordx4 v232, %[rx], %[sx] offen lds\n\t" \
  "ds_read_b128 v[12:15], v223 offset:0\n\t" \
  "s_waitcnt lgkmcnt(8)\n\t" \
  "v_mfma_f32_16x16x32_bf16 v[152:155], v[184:187], v[0:3], 0\n\t" \
  "ds_read_b64_tr_b16 v[200:201], v224 offset:46080\n\t" \
  "v_mfma_f32_16x16x32_bf16 v[156:159], v[184:187], v[4:7], 0\n\t" \
  "s_add_u32 m0, %[m2], 122880\n\t" \
  "s_nop 0\n\t" \
  "buffer_load_dwordx4 v234, %[ra], %[sw] offen lds\n\t" \
  "s_cmp_lg_u32 %[e2], %[l0]\n\t" \
  "s_cbranch_scc1 .Lp1t5_%=\n\t" \
  "s_add_u32 m0, %[m0q], 139264\n\t" \
  "s_nop 0\n\t" \
  "buffer_load_dwordx4 v236, %[ra], %[sw] offen lds\n\t" \
  ".Lp1t5_%=:\n\t" \
  "ds_read_b64_tr_b16 v[202:203], v224 offset:50688\n\t" \
  "ds_read_b64_tr_b16 v[204:205], v224 offset:46088\n\t" \
  "s_waitcnt lgkmcnt(9)\n\t" \
  "v_mfma_f32_16x16x32_bf16 v[160:163], v[188:191], v[0:3], 0\n\t" \
  "ds_read_b64_tr_b16 v[206:207], v224 offset:50696\n\t" \
  "v_mfma_f32_16x16x32_bf16 v[164:167], v[188:191], v[4:7], 0\n\t" \
  "s_add_u32 m0, %[m2], 33792\n\t" \
  "s_nop 0\n\t" \
  "buffer_load_dwordx4 v233, %[rx], %[sx] offen lds\n\t" \
  "ds_read_b64_tr_b16 v[208:209], v224 offset:46152\n\t" \
  "ds_read_b64_tr_b16 v[210:211], v224 offset:50760\n\t" \
  "s_waitcnt lgkmcnt(10)\n\t" \
  "v_mfma_f32_16x16x32_bf16 v[168:171], v[192:195], v[0:3], 0\n\t" \
  "ds_read_b64_tr_b16 v[212:213], v224 offset:46160\n\t" \
  "ds_read_b64_tr_b16 v[214:215], v224 offset:50768\n\t" \
  "v_mfma_f32_16x16x32_bf16 v[172:175], v[192:195], v[4:7], 0\n\t" \
  "s_add_u32 m0, %[m2], 123904\n\t" \
  "s_nop 0\n\t" \
  "buffer_load_dwordx4 v235, %[ra], %[sw] offen lds\n\t" \
  "ds_read_b64_tr_b16 v[216:217], v226 offset:46080\n\t" \
  "s_waitcnt lgkmcnt(11)\n\t" \
  "v_mfma_f32_16x16x32_bf16 v[176:179], v[196:199], v[0:3], 0\n\t" \
  "ds_read_b64_tr_b16 v[218:219], v226 offset:50688\n\t" \
  "s_cmp_lg_u32 %[ch], 0\n\t" \
  "s_waitcnt vmcnt(14) lgkmcnt(10)\n\t" \
  "s_cbranch_scc1 .Lp1t6_%=\n\t" \
  "s_waitcnt vmcnt(4)\n\t" \
  ".Lp1t6_%=:\n\t" \
  "s_barrier\n\t" \
  "s_waitcnt lgkmcnt(8)\n\t" \
  "v_mfma_f32_16x16x32_bf16 v[144:147], v[200:203], v[8:11], v[144:147]\n\t" \
  "ds_read_b128 v[16:19], v220 offset:16384\n\t" \
  "v_mfma_f32_16x16x32_bf16 v[148:151], v[200:203], v[12:15], v[148:151]\n\t" \
  "ds_read_b128 v[20:23], v221 offset:16384\n\t" \
  "s_waitcnt lgkmcnt(8)\n\t" \
  "v_mfma_f32_16x16x32_bf16 v[152:155], v[204:207], v[8:11], v[152:155]\n\t" \
  "s_add_u32 %[sx], %[sx], 128\n\t" \
  "s_cmp_eq_u32 %[sx], 0x480\n\t" \
  "s_cselect_b32 %[sx], 0, %[sx]\n\t" \
  "s_mul_i32 %[sw], %[sx], 0x6c0\n\t" \
  "s_add_u32 m0, %[m2], 0\n\t" \
  "s_nop 0\n\t" \
  "buffer_load_dwordx4 v232, %[rx], %[sx] offen lds\n\t" \
  "ds_read_b64_tr_b16 v[180:181], v225 offset:0\n\t" \
  "v_mfma_f32_16x16x32_bf16 v[156:159], v[204:207], v[12:15], v[156:159]\n\t" \
  "ds_read_b64_tr_b16 v[182:183], v225 offset:4608\n\t" \
  "ds_read_b64_tr_b16 v[184:185], v225 offset:8\n\t" \
  "s_waitcnt lgkmcnt(9)\n\t" \
  "v_mfma_f32_16x16x32_bf16 v[160:163], v[208:211], v[8:11], v[160:163]\n\t" \
  "ds_read_b64_tr_b16 v[186:187], v225 offset:4616\n\t" \
  "v_mfma_f32_16x16x32_bf16 v[164:167], v[208:211], v[12:15], v[164:167]\n\t" \
  "ds_read_b64_tr_b16 v[188:189], v225 offset:72\n\t" \
  "ds_read_b64_tr_b16 v[190:191], v225 offset:4680\n\t" \
  "s_waitcnt lgkmcnt(10)\n\t" \
  "v_mfma_f32_16x16x32_bf16 v[168:171], v[212:215], v[8:11], v[168:171]\n\t" \
  "s_add_u32 m0, %[m2], 141312\n\t" \
  "s_nop 0\n\t" \
  "buffer_load_dwordx4 v234, %[ra], %[sw] offen lds\n\t" \
  "s_cmp_lg_u32 %[e2], %[l0]\n\t" \
  "s_cbranch_scc1 .Lp1t7_%=\n\t" \
  "s_add_u32 m0, %[m0q], 157696\n\t" \
  "s_nop 0\n\t" \
  "buffer_load_dwordx4 v236, %[ra], %[sw] offen lds\n\t" \
  ".Lp1t7_%=:\n\t" \
  "ds_read_b64_tr_b16 v[192:193], v225 offset:80\n\t" \
  "ds_read_b64_tr_b16 v[194:195], v225 offset:4688\n\t" \
  "v_mfma_f32_16x16x32_bf16 v[172:175], v[212:215], v[12:15], v[172:175]\n\t" \
  "ds_read_b64_tr_b16 v[196:197], v227 offset:0\n\t" \
  "s_waitcnt lgkmcnt(11)\n\t" \
  "v_mfma_f32_16x16x32_bf16 v[176:179], v[216:219], v[8:11], v[176:179]\n\t" \
  "ds_read_b64_tr_b16 v[198:199], v227 offset:4608\n\t" \
  "s_waitcnt lgkmcnt(8)\n\t" \
  "v_mfma_f32_16x16x32_bf16 v[144:147], v[180:183], v[16:19], v[144:147]\n\t" \
  "ds_read_b128 v[24:27], v222 offset:16384\n\t" \
  "v_mfma_f32_16x16x32_bf16 v[148:151], v[180:183], v[20:23], v[148:151]\n\t" \
  "ds_read_b128 v[28:31], v223 offset:16384\n\t" \
  "s_waitcnt lgkmcnt(8)\n\t" \
  "v_mfma_f32_16x16x32_bf16 v[152:155], v[184:187], v[16:19], v[152:155]\n\t" \
  "s_add_u32 m0, %[m2], 1024\n\t" \
  "s_nop 0\n\t" \
  "buffer_load_dwordx4 v233, %[rx], %[sx] offen lds\n\t" \
  "ds_read_b64_tr_b16 v[200:201], v225 offset:9216\n\t" \
  "v_mfma_f32_16x16x32_bf16 v[156:159], v[184:187], v[20:23], v[156:159]\n\t" \
  "ds_read_b64_tr_b16 v[202:203], v225 offset:13824\n\t" \
  "ds_read_b64_tr_b16 v[204:205], v225 offset:9224\n\t" \
  "s_waitcnt lgkmcnt(9)\n\t" \
  "v_mfma_f32_16x16x32_bf16 v[160:163], v[188:191], v[16:19], v[160:163]\n\t" \
  "ds_read_b64_tr_b16 v[206:207], v225 offset:13832\n\t" \
  "v_mfma_f32_16x16x32_bf16 v[164:167], v[188:191], v[20:23], v[164:167]\n\t" \
  "ds_read_b64_tr_b16 v[208:209], v225 offset:9288\n\t" \
  "ds_read_b64_tr_b16 v[210:211], v225 offset:13896\n\t" \
  "s_waitcnt lgkmcnt(10)\n\t" \
  "v_mfma_f32_16x16x32_bf16 v[168:171], v[192:195], v[16:19], v[168:171]\n\t" \
  "s_add_u32 m0, %[m2], 142336\n\t" \
  "s_nop 0\n\t" \
  "buffer_load_dwordx4 v235, %[ra], %[sw] offen lds\n\t" \
  "ds_read_b64_tr_b16 v[212:213], v225 offset:9296\n\t" \
  "ds_read_b64_tr_b16 v[214:215], v225 offset:13904\n\t" \
  "v_mfma_f32_16x16x32_bf16 v[172:175], v[192:195], v[20:23], v[172:175]\n\t" \
  "ds_read_b64_tr_b16 v[216:217], v227 offset:9216\n\t" \
  "s_waitcnt lgkmcnt(11)\n\t" \
  "v_mfma_f32_16x16x32_bf16 v[176:179], v[196:199], v[16:19], v[176:179]\n\t" \
  "ds_read_b64_tr_b16 v[218:219], v227 offset:13824\n\t" \
  "s_waitcnt vmcnt(4) lgkmcnt(10)\n\t" \
  "s_barrier\n\t" \
  "s_waitcnt lgkmcnt(8)\n\t" \
  "v_mfma_f32_16x16x32_bf16 v[144:147], v[200:203], v[24:27], v[144:147]\n\t" \
  "ds_read_b128 v[32:35], v220 offset:32768\n\t" \
  "v_mfma_f32_16x16x32_bf16 v[148:151], v[200:203], v[28:31], v[148:151]\n\t" \
  "ds_read_b128 v[36:39], v221 offset:32768\n\t" \
  "s_waitcnt lgkmcnt(8)\n\t" \
  "v_mfma_f32_16x16x32_bf16 v[152:155], v[204:207], v[24:27], v[152:155]\n\t" \
  "s_add_u32 %[sx], %[sx], 128\n\t" \
  "s_cmp_eq_u32 %[sx], 0x480\n\t" \
  "s_cselect_b32 %[sx], 0, %[sx]\n\t" \
  "s_mul_i32 %[sw], %[sx], 0x6c0\n\t" \
  "s_add_u32 m0, %[m2], 16384\n\t" \
  "s_nop 0\n\t" \
  "buffer_load_dwordx4 v232, %[rx], %[sx] offen lds\n\t" \
  "ds_read_b64_tr_b16 v[180:181], v225 offset:18432\n\t" \
  "v_mfma_f32_16x16x32_bf16 v[156:159], v[204:207], v[28:31], v[156:159]\n\t" \
  "ds_read_b64_tr_b16 v[182:183], v225 offset:23040\n\t" \
  "ds_read_b64_tr_b16 v[184:185], v225 offset:18440\n\t" \
  "s_waitcnt lgkmcnt(9)\n\t" \
  "v_mfma_f32_16x16x32_bf16 v[160:163], v[208:211], v[24:27], v[160:163]\n\t" \
  "ds_read_b64_tr_b16 v[186:187], v225 offset:23048\n\t" \
  "v_mfma_f32_16x16x32_bf16 v[164:167], v[208:211], v[28:31], v[164:167]\n\t" \
  "ds_read_b64_tr_b16 v[188:189], v225 offset:18504\n\t" \
  "ds_read_b64_tr_b16 v[190:191], v225 offset:23112\n\t" \
  "s_waitcnt lgkmcnt(10)\n\t" \
  "v_mfma_f32_16x16x32_bf16 v[168:171], v[212:215], v[24:27], v[168:171]\n\t" \
  "s_add_u32 m0, %[m2], 86016\n\t" \
  "s_nop 0\n\t" \
  "buffer_load_dwordx4 v234, %[ra], %[sw] offen lds\n\t" \
  "s_cmp_lg_u32 %[e2], %[l0]\n\t" \
  "s_cbranch_scc1 .Lp1t8_%=\n\t" \
  "s_add_u32 m0, %[m0q], 102400\n\t" \
  "s_nop 0\n\t" \
  "buffer_load_dwordx4 v236, %[ra], %[sw] offen lds\n\t" \
  ".Lp1t8_%=:\n\t" \
  "ds_read_b64_tr_b16 v[192:193], v225 offset:18512\n\t" \
  "ds_read_b64_tr_b16 v[194:195], v225 offset:23120\n\t" \
  "v_mfma_f32_16x16x32_bf16 v[172:175], v[212:215], v[28:31], v[172:175]\n\t" \
  "ds_read_b64_tr_b16 v[196:197], v227 offset:18432\n\t" \
  "s_waitcnt lgkmcnt(11)\n\t" \
  "v_mfma_f32_16x16x32_bf16 v[176:179], v[216:219], v[24:27], v[176:179]\n\t" \
  "ds_read_b64_tr_b16 v[198:199], v227 offset:23040\n\t" \
  "s_waitcnt lgkmcnt(8)\n\t" \
  "v_mfma_f32_16x16x32_bf16 v[144:147], v[180:183], v[32:35], v[144:147]\n\t" \
  "ds_read_b128 v[40:43], v222 offset:32768\n\t" \
  "v_mfma_f32_16x16x32_bf16 v[148:151], v[180:183], v[36:39], v[148:151]\n\t" \
  "ds_read_b128 v[44:47], v223 offset:32768\n\t" \
  "s_waitcnt lgkmcnt(8)\n\t" \
  "v_mfma_f32_16x16x32_bf16 v[152:155], v[184:187], v[32:35], v[152:155]\n\t" \
  "s_add_u32 m0, %[m2], 17408\n\t" \
  "s_nop 0\n\t" \
  "buffer_load_dwordx4 v233, %[rx], %[sx] offen lds\n\t" \
  "ds_read_b64_tr_b16 v[200:201], v225 offset:27648\n\t" \
  "v_mfma_f32_16x16x32_bf16 v[156:159], v[184:187], v[36:39], v[156:159]\n\t" \
  "ds_read_b64_tr_b16 v[202:203], v225 offset:32256\n\t" \
  "ds_read_b64_tr_b16 v[204:205], v225 offset:27656\n\t" \
  "s_waitcnt lgkmcnt(9)\n\t" \
  "v_mfma_f32_16x16x32_bf16 v[160:163], v[188:191], v[32:35], v[160:163]\n\t" \
  "ds_read_b64_tr_b16 v[206:207], v225 offset:32264\n\t" \
  "v_mfma_f32_16x16x32_bf16 v[164:167], v[188:191], v[36:39], v[164:167]\n\t" \
  "ds_read_b64_tr_b16 v[208:209], v225 offset:27720\n\t" \
  "ds_read_b64_tr_b16 v[210:211], v225 offset:32328\n\t" \
  "s_waitcnt lgkmcnt(10)\n\t" \
  "v_mfma_f32_16x16x32_bf16 v[168:171], v[192:195], v[32:35], v[168:171]\n\t" \
  "s_add_u32 m0, %[m2], 87040\n\t" \
  "s_nop 0\n\t" \
  "buffer_load_dwordx4 v235, %[ra], %[sw] offen lds\n\t" \
  "ds_read_b64_tr_b16 v[212:213], v225 offset:27728\n\t" \
  "ds_read_b64_tr_b16 v[214:215], v225 offset:32336\n\t" \
  "v_mfma_f32_16x16x32_bf16 v[172:175], v[192:195], v[36:39], v[172:175]\n\t" \
  "ds_read_b64_tr_b16 v[216:217], v227 offset:27648\n\t" \
  "s_waitcnt lgkmcnt(11)\n\t" \
  "v_mfma_f32_16x16x32_bf16 v[176:179], v[196:199], v[32:35], v[176:179]\n\t" \
  "ds_read_b64_tr_b16 v[218:219], v227 offset:32256\n\t" \
  "s_cmp_eq_u32 %[ch], 0\n\t" \
  "s_cbranch_scc1 .Lp1t9_%=\n\t" \
  "v_mov_b32 v239, 0\n\t" \
  "v_dot2c_f32_bf16 v239, v104, v108\n\t" \
  "v_dot2c_f32_bf16 v239, v105, v109\n\t" \
  "v_dot2c_f32_bf16 v239, v106, v110\n\t" \
  "v_dot2c_f32_bf16 v239, v107, v111\n\t" \
  "v_dot2c_f32_bf16 v239, v112, v116\n\t" \
  "v_dot2c_f32_bf16 v239, v113, v117\n\t" \
  "v_dot2c_f32_bf16 v239, v114, v118\n\t" \
  "v_dot2c_f32_bf16 v239, v115, v119\n\t" \
  "v_dot2c_f32_bf16 v239, v120, v124\n\t" \
  "v_dot2c_f32_bf16 v239, v121, v125\n\t" \
  "v_dot2c_f32_bf16 v239, v122, v126\n\t" \
  "v_dot2c_f32_bf16 v239, v123, v127\n\t" \
  "v_dot2c_f32_bf16 v239, v128, v132\n\t" \
  "v_dot2c_f32_bf16 v239, v129, v133\n\t" \
  "v_dot2c_f32_bf16 v239, v130, v134\n\t" \
  "v_dot2c_f32_bf16 v239, v131, v135\n\t" \
  "v_dot2c_f32_bf16 v239, v136, v140\n\t" \
  "v_dot2c_f32_bf16 v239, v137, v141\n\t" \
  "v_dot2c_f32_bf16 v239, v138, v142\n\t" \
  "v_dot2c_f32_bf16 v239, v139, v143\n\t" \
  ".Lp1t9_%=:\n\t" \
  "s_waitcnt vmcnt(4) lgkmcnt(10)\n\t" \
  "s_barrier\n\t" \
  "s_waitcnt lgkmcnt(8)\n\t" \
  "v_mfma_f32_16x16x32_bf16 v[144:147], v[200:203], v[40:43], v[144:147]\n\t" \
  "ds_read_b128 v[48:51], v220 offset:0\n\t" \
  "v_mfma_f32_16x16x32_bf16 v[148:151], v[200:203], v[44:47], v[148:151]\n\t" \
  "ds_read_b128 v[52:55], v221 offset:0\n\t" \
  "s_waitcnt lgkmcnt(8)\n\t" \
  "v_mfma_f32_16x16x32_bf16 v[152:155], v[204:207], v[40:43], v[152:155]\n\t" \
  "s_add_u32 %[sx], %[sx], 128\n\t" \
  "s_cmp_eq_u32 %[sx], 0x480\n\t" \
  "s_cselect_b32 %[sx], 0, %[sx]\n\t" \
  "s_mul_i32 %[sw], %[sx], 0x6c0\n\t" \
  "s_add_u32 m0, %[m2], 32768\n\t" \
  "s_nop 0\n\t" \
  "buffer_load_dwordx4 v232, %[rx], %[sx] offen lds\n\t" \
  "ds_read_b64_tr_b16 v[180:181], v225 offset:36864\n\t" \
  "v_mfma_f32_16x16x32_bf16 v[156:159], v[204:207], v[44:47], v[156:159]\n\t" \
  "ds_read_b64_tr_b16 v[182:183], v225 offset:41472\n\t" \
  "ds_read_b64_tr_b16 v[184:185], v225 offset:36872\n\t" \
  "s_waitcnt lgkmcnt(9)\n\t" \
  "v_mfma_f32_16x16x32_bf16 v[160:163], v[208:211], v[40:43], v[160:163]\n\t" \
  "ds_read_b64_tr_b16 v[186:187], v225 offset:41480\n\t" \
  "v_mfma_f32_16x16x32_bf16 v[164:167], v[208:211], v[44:47], v[164:167]\n\t" \
  "ds_read_b64_tr_b16 v[188:189], v225 offset:36936\n\t" \
  "ds_read_b64_tr_b16 v[190:191], v225 offset:41544\n\t" \
  "s_waitcnt lgkmcnt(10)\n\t" \
  "v_mfma_f32_16x16x32_bf16 v[168:171], v[212:215], v[40:43], v[168:171]\n\t" \
  "s_add_u32 m0, %[m2], 104448\n\t" \
  "s_nop 0\n\t" \
  "buffer_load_dwordx4 v234, %[ra], %[sw] offen lds\n\t" \
  "s_cmp_lg_u32 %[e2], %[l0]\n\t" \
  "s_cbranch_scc1 .Lp1t10_%=\n\t" \
  "s_add_u32 m0, %[m0q], 120832\n\t" \
  "s_nop 0\n\t" \
  "buffer_load_dwordx4 v236, %[ra], %[sw] offen lds\n\t" \
  ".Lp1t10_%=:\n\t" \
  "ds_read_b64_tr_b16 v[192:193], v225 offset:36944\n\t" \
  "ds_read_b64_tr_b16 v[194:195], v225 offset:41552\n\t" \
  "v_mfma_f32_16x16x32_bf16 v[172:175], v[212:215], v[44:47], v[172:175]\n\t" \
  "ds_read_b64_tr_b16 v[196:197], v227 offset:36864\n\t" \
  "s_waitcnt lgkmcnt(11)\n\t" \
  "v_mfma_f32_16x16x32_bf16 v[176:179], v[216:219], v[40:43], v[176:179]\n\t" \
  "ds_read_b64_tr_b16 v[198:199], v227 offset:41472\n\t" \
  "s_waitcnt lgkmcnt(8)\n\t" \
  "v_mfma_f32_16x16x32_bf16 v[144:147], v[180:183], v[48:51], v[144:147]\n\t" \
  "ds_read_b128 v[56:59], v222 offset:0\n\t" \
  "v_mfma_f32_16x16x32_bf16 v[148:151], v[180:183], v[52:55], v[148:151]\n\t" \
  "ds_read_b128 v[60:63], v223 offset:0\n\t" \
  "s_waitcnt lgkmcnt(8)\n\t" \
  "v_mfma_f32_16x16x32_bf16 v[152:155], v[184:187], v[48:51], v[152:155]\n\t" \
  "s_add_u32 m0, %[m2], 33792\n\t" \
  "s_nop 0\n\t" \
  "buffer_load_dwordx4 v233, %[rx], %[sx] offen lds\n\t" \
  "ds_read_b64_tr_b16 v[200:201], v225 offset:46080\n\t" \
  "v_mfma_f32_16x16x32_bf16 v[156:159], v[184:187], v[52:55], v[156:159]\n\t" \
  "ds_read_b64_tr_b16 v[202:203], v225 offset:50688\n\t" \
  "ds_read_b64_tr_b16 v[204:205], v225 offset:46088\n\t" \
  "s_waitcnt lgkmcnt(9)\n\t" \
  "v_mfma_f32_16x16x32_bf16 v[160:163], v[188:191], v[48:51], v[160:163]\n\t" \
  "ds_read_b64_tr_b16 v[206:207], v225 offset:50696\n\t" \
  "v_mfma_f32_16x16x32_bf16 v[164:167], v[188:191], v[52:55], v[164:167]\n\t" \
  "ds_read_b64_tr_b16 v[208:209], v225 offset:46152\n\t" \
  "ds_read_b64_tr_b16 v[210:211], v225 offset:50760\n\t" \
  "s_waitcnt lgkmcnt(10)\n\t" \
  "v_mfma_f32_16x16x32_bf16 v[168:171], v[192:195], v[48:51], v[168:171]\n\t" \
  "s_add_u32 m0, %[m2], 105472\n\t" \
  "s_nop 0\n\t" \
  "buffer_load_dwordx4 v235, %[ra], %[sw] offen lds\n\t" \
  "ds_read_b64_tr_b16 v[212:213], v225 offset:46160\n\t" \
  "ds_read_b64_tr_b16 v[214:215], v225 offset:50768\n\t" \
  "v_mfma_f32_16x16x32_bf16 v[172:175], v[192:195], v[52:55], v[172:175]\n\t" \
  "ds_read_b64_tr_b16 v[216:217], v227 offset:46080\n\t" \
  "s_waitcnt lgkmcnt(11)\n\t" \
  "v_mfma_f32_16x16x32_bf16 v[176:179], v[196:199], v[48:51], v[176:179]\n\t" \
  "ds_read_b64_tr_b16 v[218:219], v227 offset:50688\n\t" \
  "s_waitcnt vmcnt(4) lgkmcnt(10)\n\t" \
  "s_barrier\n\t" \
  "s_waitcnt lgkmcnt(8)\n\t" \
  "v_mfma_f32_16x16x32_bf16 v[144:147], v[200:203], v[56:59], v[144:147]\n\t" \
  "ds_read_b128 v[64:67], v220 offset:16384\n\t" \
  "v_mfma_f32_16x16x32_bf16 v[148:151], v[200:203], v[60:63], v[148:151]\n\t" \
  "ds_read_b128 v[68:71], v221 offset:16384\n\t" \
  "s_waitcnt lgkmcnt(8)\n\t" \
  "v_mfma_f32_16x16x32_bf16 v[152:155], v[204:207], v[56:59], v[152:155]\n\t" \
  "s_add_u32 %[sx], %[sx], 128\n\t" \
  "s_cmp_eq_u32 %[sx], 0x480\n\t" \
  "s_cselect_b32 %[sx], 0, %[sx]\n\t" \
  "s_mul_i32 %[sw], %[sx], 0x6c0\n\t" \
  "s_add_u32 m0, %[m2], 0\n\t" \
  "s_nop 0\n\t" \
  "buffer_load_dwordx4 v232, %[rx], %[sx] offen lds\n\t" \
  "ds_read_b64_tr_b16 v[180:181], v224 offset:36864\n\t" \
  "v_mfma_f32_16x16x32_bf16 v[156:159], v[204:207], v[60:63], v[156:159]\n\t" \
  "ds_read_b64_tr_b16 v[182:183], v224 offset:41472\n\t" \
  "ds_read_b64_tr_b16 v[184:185], v224 offset:36872\n\t" \
  "s_waitcnt lgkmcnt(9)\n\t" \
  "v_mfma_f32_16x16x32_bf16 v[160:163], v[208:211], v[56:59], v[160:163]\n\t" \
  "ds_read_b64_tr_b16 v[186:187], v224 offset:41480\n\t" \
  "v_mfma_f32_16x16x32_bf16 v[164:167], v[208:211], v[60:63], v[164:167]\n\t" \
  "ds_read_b64_tr_b16 v[188:189], v224 offset:36936\n\t" \
  "ds_read_b64_tr_b16 v[190:191], v224 offset:41544\n\t" \
  "s_waitcnt lgkmcnt(10)\n\t" \
  "v_mfma_f32_16x16x32_bf16 v[168:171], v[212:215], v[56:59], v[168:171]\n\t" \
  "s_add_u32 m0, %[m2], 122880\n\t" \
  "s_nop 0\n\t" \
  "buffer_load_dwordx4 v234, %[ra], %[sw] offen lds\n\t" \
  "s_cmp_lg_u32 %[e2], %[l0]\n\t" \
  "s_cbranch_scc1 .Lp1t11_%=\n\t" \
  "s_add_u32 m0, %[m0q], 139264\n\t" \
  "s_nop 0\n\t" \
  "buffer_load_dwordx4 v236, %[ra], %[sw] offen lds\n\t" \
  ".Lp1t11_%=:\n\t" \
  "ds_read_b64_tr_b16 v[192:193], v224 offset:36944\n\t" \
  "ds_read_b64_tr_b16 v[194:195], v224 offset:41552\n\t" \
  "v_mfma_f32_16x16x32_bf16 v[172:175], v[212:215], v[60:63], v[172:175]\n\t" \
  "ds_read_b64_tr_b16 v[196:197], v226 offset:36864\n\t" \
  "s_waitcnt lgkmcnt(11)\n\t" \
  "v_mfma_f32_16x16x32_bf16 v[176:179], v[216:219], v[56:59], v[176:179]\n\t" \
  "ds_read_b64_tr_b16 v[198:199], v226 offset:41472\n\t" \
  "s_waitcnt lgkmcnt(8)\n\t" \
  "v_mfma_f32_16x16x32_bf16 v[144:147], v[180:183], v[64:67], v[144:147]\n\t" \
  "ds_read_b128 v[72:75], v222 offset:16384\n\t" \
  "v_mfma_f32_16x16x32_bf16 v[148:151], v[180:183], v[68:71], v[148:151]\n\t" \
  "ds_read_b128 v[76:79], v223 offset:16384\n\t" \
  "s_waitcnt lgkmcnt(8)\n\t" \
  "v_mfma_f32_16x16x32_bf16 v[152:155], v[184:187], v[64:67], v[152:155]\n\t" \
  "s_add_u32 m0, %[m2], 1024\n\t" \
  "s_nop 0\n\t" \
  "buffer_load_dwordx4 v233, %[rx], %[sx] offen lds\n\t" \
  "ds_read_b64_tr_b16 v[200:201], v224 offset:46080\n\t" \
  "v_mfma_f32_16x16x32_bf16 v[156:159], v[184:187], v[68:71], v[156:159]\n\t" \
  "ds_read_b64_tr_b16 v[202:203], v224 offset:50688\n\t" \
  "ds_read_b64_tr_b16 v[204:205], v224 offset:46088\n\t" \
  "s_waitcnt lgkmcnt(9)\n\t" \
  "v_mfma_f32_16x16x32_bf16 v[160:163], v[188:191], v[64:67], v[160:163]\n\t" \
  "ds_read_b64_tr_b16 v[206:207], v224 offset:50696\n\t" \
  "v_mfma_f32_16x16x32_bf16 v[164:167], v[188:191], v[68:71], v[164:167]\n\t" \
  "ds_read_b64_tr_b16 v[208:209], v224 offset:46152\n\t" \
  "ds_read_b64_tr_b16 v[210:211], v224 offset:50760\n\t" \
  "s_waitcnt lgkmcnt(10)\n\t" \
  "v_mfma_f32_16x16x32_bf16 v[168:171], v[192:195], v[64:67], v[168:171]\n\t" \
  "s_add_u32 m0, %[m2], 123904\n\t" \
  "s_nop 0\n\t" \
  "buffer_load_dwordx4 v235, %[ra], %[sw] offen lds\n\t" \
  "ds_read_b64_tr_b16 v[212:213], v224 offset:46160\n\t" \
  "ds_read_b64_tr_b16 v[214:215], v224 offset:50768\n\t" \
  "v_mfma_f32_16x16x32_bf16 v[172:175], v[192:195], v[68:71], v[172:175]\n\t" \
  "ds_read_b64_tr_b16 v[216:217], v226 offset:46080\n\t" \
  "s_waitcnt lgkmcnt(11)\n\t" \
  "v_mfma_f32_16x16x32_bf16 v[176:179], v[196:199], v[64:67], v[176:179]\n\t" \
  "ds_read_b64_tr_b16 v[218:219], v226 offset:50688\n\t" \
  "s_waitcnt vmcnt(4) lgkmcnt(10)\n\t" \
  "s_barrier\n\t" \
  "s_waitcnt lgkmcnt(8)\n\t" \
  "v_mfma_f32_16x16x32_bf16 v[144:147], v[200:203], v[72:75], v[144:147]\n\t" \
  "ds_read_b128 v[80:83], v220 offset:32768\n\t" \
  "v_mfma_f32_16x16x32_bf16 v[148:151], v[200:203], v[76:79], v[148:151]\n\t" \
  "ds_read_b128 v[84:87], v221 offset:32768\n\t" \
  "s_waitcnt lgkmcnt(8)\n\t" \
  "v_mfma_f32_16x16x32_bf16 v[152:155], v[204:207], v[72:75], v[152:155]\n\t" \
  "s_add_u32 %[sx], %[sx], 128\n\t" \
  "s_cmp_eq_u32 %[sx], 0x480\n\t" \
  "s_cselect_b32 %[sx], 0, %[sx]\n\t" \
  "s_mul_i32 %[sw], %[sx], 0x6c0\n\t" \
  "s_add_u32 m0, %[m2], 16384\n\t" \
  "s_nop 0\n\t" \
  "buffer_load_dwordx4 v232, %[rx], %[sx] offen lds\n\t" \
  "ds_read_b64_tr_b16 v[180:181], v225 offset:0\n\t" \
  "v_mfma_f32_16x16x32_bf16 v[156:159], v[204:207], v[76:79], v[156:159]\n\t" \
  "ds_read_b64_tr_b16 v[182:183], v225 offset:4608\n\t" \
  "ds_read_b64_tr_b16 v[184:185], v225 offset:8\n\t" \
  "s_waitcnt lgkmcnt(9)\n\t" \
  "v_mfma_f32_16x16x32_bf16 v[160:163], v[208:211], v[72:75], v[160:163]\n\t" \
  "ds_read_b64_tr_b16 v[186:187], v225 offset:4616\n\t" \
  "v_mfma_f32_16x16x32_bf16 v[164:167], v[208:211], v[76:79], v[164:167]\n\t" \
  "ds_read_b64_tr_b16 v[188:189], v225 offset:72\n\t" \
  "ds_read_b64_tr_b16 v[190:191], v225 offset:4680\n\t" \
  "s_waitcnt lgkmcnt(10)\n\t" \
  "v_mfma_f32_16x16x32_bf16 v[168:171], v[212:215], v[72:75], v[168:171]\n\t" \
  "s_add_u32 m0, %[m2], 141312\n\t" \
  "s_nop 0\n\t" \
  "buffer_load_dwordx4 v234, %[ra], %[sw] offen lds\n\t" \
  "s_cmp_lg_u32 %[e2], %[l0]\n\t" \
  "s_cbranch_scc1 .Lp1t12_%=\n\t" \
  "s_add_u32 m0, %[m0q], 157696\n\t" \
  "s_nop 0\n\t" \
  "buffer_load_dwordx4 v236, %[ra], %[sw] offen lds\n\t" \
  ".Lp1t12_%=:\n\t" \
  "ds_read_b64_tr_b16 v[192:193], v225 offset:80\n\t" \
  "ds_read_b64_tr_b16 v[194:195], v225 offset:4688\n\t" \
  "v_mfma_f32_16x16x32_bf16 v[172:175], v[212:215], v[76:79], v[172:175]\n\t" \
  "ds_read_b64_tr_b16 v[196:197], v227 offset:0\n\t" \
  "s_waitcnt lgkmcnt(11)\n\t" \
  "v_mfma_f32_16x16x32_bf16 v[176:179], v[216:219], v[72:75], v[176:179]\n\t" \
  "ds_read_b64_tr_b16 v[198:199], v227 offset:4608\n\t" \
  "s_waitcnt lgkmcnt(8)\n\t" \
  "v_mfma_f32_16x16x32_bf16 v[144:147], v[180:183], v[80:83], v[144:147]\n\t" \
  "ds_read_b128 v[88:91], v222 offset:32768\n\t" \
  "v_mfma_f32_16x16x32_bf16 v[148:151], v[180:183], v[84:87], v[148:151]\n\t" \
  "ds_read_b128 v[92:95], v223 offset:32768\n\t" \
  "s_waitcnt lgkmcnt(8)\n\t" \
  "v_mfma_f32_16x16x32_bf16 v[152:155], v[184:187], v[80:83], v[152:155]\n\t" \
  "s_add_u32 m0, %[m2], 17408\n\t" \
  "s_nop 0\n\t" \
  "buffer_load_dwordx4 v233, %[rx], %[sx] offen lds\n\t" \
  "ds_read_b64_tr_b16 v[200:201], v225 offset:9216\n\t" \
  "v_mfma_f32_16x16x32_bf16 v[156:159], v[184:187], v[84:87], v[156:159]\n\t" \
  "ds_read_b64_tr_b16 v[202:203], v225 offset:13824\n\t" \
  "ds_read_b64_tr_b16 v[204:205], v225 offset:9224\n\t" \
  "s_waitcnt lgkmcnt(9)\n\t" \
  "v_mfma_f32_16x16x32_bf16 v[160:163], v[188:191], v[80:83], v[160:163]\n\t" \
  "ds_read_b64_tr_b16 v[206:207], v225 offset:13832\n\t" \
  "v_mfma_f32_16x16x32_bf16 v[164:167], v[188:191], v[84:87], v[164:167]\n\t" \
  "ds_read_b64_tr_b16 v[208:209], v225 offset:9288\n\t" \
  "ds_read_b64_tr_b16 v[210:211], v225 offset:13896\n\t" \
  "s_waitcnt lgkmcnt(10)\n\t" \
  "v_mfma_f32_16x16x32_bf16 v[168:171], v[192:195], v[80:83], v[168:171]\n\t" \
  "s_add_u32 m0, %[m2], 142336\n\t" \
  "s_nop 0\n\t" \
  "buffer_load_dwordx4 v235, %[ra], %[sw] offen lds\n\t" \
  "ds_read_b64_tr_b16 v[212:213], v225 offset:9296\n\t" \
  "ds_read_b64_tr_b16 v[214:215], v225 offset:13904\n\t" \
  "v_mfma_f32_16x16x32_bf16 v[172:175], v[192:195], v[84:87], v[172:175]\n\t" \
  "ds_read_b64_tr_b16 v[216:217], v227 offset:9216\n\t" \
  "s_waitcnt lgkmcnt(11)\n\t" \
  "v_mfma_f32_16x16x32_bf16 v[176:179], v[196:199], v[80:83], v[176:179]\n\t" \
  "ds_read_b64_tr_b16 v[218:219], v227 offset:13824\n\t" \
  "s_waitcnt vmcnt(4) lgkmcnt(10)\n\t" \
  "s_barrier\n\t" \
  "s_waitcnt lgkmcnt(8)\n\t" \
  "v_mfma_f32_16x16x32_bf16 v[144:147], v[200:203], v[88:91], v[144:147]\n\t" \
  "ds_read_b128 v[96:99], v220 offset:0\n\t" \
  "v_mfma_f32_16x16x32_bf16 v[148:151], v[200:203], v[92:95], v[148:151]\n\t" \
  "s_add_u32 %[sx], %[sx], 128\n\t" \
  "s_cmp_eq_u32 %[sx], 0x480\n\t" \
  "s_cselect_b32 %[sx], 0, %[sx]\n\t" \
  "s_mul_i32 %[sw], %[sx], 0x6c0\n\t" \
  "s_add_u32 m0, %[m2], 32768\n\t" \
  "s_nop 0\n\t" \
  "buffer_load_dwordx4 v232, %[rx], %[sx] offen lds\n\t" \
  "ds_read_b128 v[100:103], v221 offset:0\n\t" \
  "s_waitcnt lgkmcnt(8)\n\t" \
  "v_mfma_f32_16x16x32_bf16 v[152:155], v[204:207], v[88:91], v[152:155]\n\t" \
  "ds_read_b64_tr_b16 v[180:181], v225 offset:18432\n\t" \
  "v_mfma_f32_16x16x32_bf16 v[156:159], v[204:207], v[92:95], v[156:159]\n\t" \
  "ds_read_b64_tr_b16 v[182:183], v225 offset:23040\n\t" \
  "ds_read_b64_tr_b16 v[184:185], v225 offset:18440\n\t" \
  "s_waitcnt lgkmcnt(9)\n\t" \
  "v_mfma_f32_16x16x32_bf16 v[160:163], v[208:211], v[88:91], v[160:163]\n\t" \
  "s_add_u32 m0, %[m2], 86016\n\t" \
  "s_nop 0\n\t" \
  "buffer_load_dwordx4 v234, %[ra], %[sw] offen lds\n\t" \
  "s_cmp_lg_u32 %[e2], %[l0]\n\t" \
  "s_cbranch_scc1 .Lp1t13_%=\n\t" \
  "s_add_u32 m0, %[m0q], 102400\n\t" \
  "s_nop 0\n\t" \
  "buffer_load_dwordx4 v236, %[ra], %[sw] offen lds\n\t" \
  ".Lp1t13_%=:\n\t" \
  "ds_read_b64_tr_b16 v[186:187], v225 offset:23048\n\t" \
  "v_mfma_f32_16x16x32_bf16 v[164:167], v[208:211], v[92:95], v[164:167]\n\t" \
  "ds_read_b64_tr_b16 v[188:189], v225 offset:18504\n\t" \
  "ds_read_b64_tr_b16 v[190:191], v225 offset:23112\n\t" \
  "s_waitcnt lgkmcnt(10)\n\t" \
  "v_mfma_f32_16x16x32_bf16 v[168:171], v[212:215], v[88:91], v[168:171]\n\t" \
  "ds_read_b64_tr_b16 v[192:193], v225 offset:18512\n\t" \
  "ds_read_b64_tr_b16 v[194:195], v225 offset:23120\n\t" \
  "v_mfma_f32_16x16x32_bf16 v[172:175], v[212:215], v[92:95], v[172:175]\n\t" \
  "s_add_u32 m0, %[m2], 33792\n\t" \
  "s_nop 0\n\t" \
  "buffer_load_dwordx4 v233, %[rx], %[sx] offen lds\n\t" \
  "ds_read_b64_tr_b16 v[196:197], v227 offset:18432\n\t" \
  "s_waitcnt lgkmcnt(11)\n\t" \
  "v_mfma_f32_16x16x32_bf16 v[176:179], v[216:219], v[88:91], v[176:179]\n\t" \
  "ds_read_b64_tr_b16 v[198:199], v227 offset:23040\n\t" \
  "s_waitcnt lgkmcnt(8)\n\t" \
  "v_mfma_f32_16x16x32_bf16 v[144:147], v[180:183], v[96:99], v[144:147]\n\t" \
  "ds_read_b128 v[104:107], v222 offset:0\n\t" \
  "v_mfma_f32_16x16x32_bf16 v[148:151], v[180:183], v[100:103], v[148:151]\n\t" \
  "s_add_u32 m0, %[m2], 87040\n\t" \
  "s_nop 0\n\t" \
  "buffer_load_dwordx4 v235, %[ra], %[sw] offen lds\n\t" \
  "ds_read_b128 v[108:111], v223 offset:0\n\t" \
  "s_waitcnt lgkmcnt(8)\n\t" \
  "v_mfma_f32_16x16x32_bf16 v[152:155], v[184:187], v[96:99], v[152:155]\n\t" \
  "ds_read_b64_tr_b16 v[200:201], v225 offset:27648\n\t" \
  "v_mfma_f32_16x16x32_bf16 v[156:159], v[184:187], v[100:103], v[156:159]\n\t" \
  "ds_read_b64_tr_b16 v[202:203], v225 offset:32256\n\t" \
  "ds_read_b64_tr_b16 v[204:205], v225 offset:27656\n\t" \
  "s_waitcnt lgkmcnt(9)\n\t" \
  "v_mfma_f32_16x16x32_bf16 v[160:163], v[188:191], v[96:99], v[160:163]\n\t" \
  "s_add_u32 m0, %[m1], 49152\n\t" \
  "s_nop 0\n\t" \
  "buffer_load_dwordx4 v237, %[rb], %[sb] offen lds\n\t" \
  "s_cmp_lg_u32 %[m1], %[l0]\n\t" \
  "s_cbranch_scc1 .Lp1t14_%=\n\t" \
  "s_add_u32 m0, %[l0], 57344\n\t" \
  "s_nop 0\n\t" \
  "buffer_load_dwordx4 v238, %[rb], %[sb] offen lds\n\t" \
  ".Lp1t14_%=:\n\t" \
  "ds_read_b64_tr_b16 v[206:207], v225 offset:32264\n\t" \
  "v_mfma_f32_16x16x32_bf16 v[164:167], v[188:191], v[100:103], v[164:167]\n\t" \
  "ds_read_b64_tr_b16 v[208:209], v225 offset:27720\n\t" \
  "ds_read_b64_tr_b16 v[210:211], v225 offset:32328\n\t" \
  "s_waitcnt lgkmcnt(10)\n\t" \
  "v_mfma_f32_16x16x32_bf16 v[168:171], v[192:195], v[96:99], v[168:171]\n\t" \
  "ds_read_b64_tr_b16 v[212:213], v225 offset:27728\n\t" \
  "ds_read_b64_tr_b16 v[214:215], v225 offset:32336\n\t" \
  "v_mfma_f32_16x16x32_bf16 v[172:175], v[192:195], v[100:103], v[172:175]\n\t" \
  "s_add_u32 %[sb], %[sb], 0x36000\n\t" \
  "s_cmp_eq_u32 %[sb], 0x1e6000\n\t" \
  "s_cselect_b32 %[sb], 0, %[sb]\n\t" \
  "s_add_u32 m0, %[m1], 58368\n\t" \
  "s_nop 0\n\t" \
  "buffer_load_dwordx4 v237, %[rb], %[sb] offen lds\n\t" \
  "s_cmp_lg_u32 %[m1], %[l0]\n\t" \
  "s_cbranch_scc1 .Lp1t15_%=\n\t" \
  "s_add_u32 m0, %[l0], 66560\n\t" \
  "s_nop 0\n\t" \
  "buffer_load_dwordx4 v238, %[rb], %[sb] offen lds\n\t" \
  ".Lp1t15_%=:\n\t" \
  "ds_read_b64_tr_b16 v[216:217], v227 offset:27648\n\t" \
  "s_waitcnt lgkmcnt(11)\n\t" \
  "v_mfma_f32_16x16x32_bf16 v[176:179], v[196:199], v[96:99], v[176:179]\n\t" \
  "ds_read_b64_tr_b16 v[218:219], v227 offset:32256\n\t" \
  "s_waitcnt vmcnt(6) lgkmcnt(10)\n\t" \
  "s_barrier\n\t" \
  "s_waitcnt lgkmcnt(8)\n\t" \
  "v_mfma_f32_16x16x32_bf16 v[144:147], v[200:203], v[104:107], v[144:147]\n\t" \
  "ds_read_b128 v[112:115], v220 offset:16384\n\t" \
  "v_mfma_f32_16x16x32_bf16 v[148:151], v[200:203], v[108:111], v[148:151]\n\t" \
  "ds_read_b128 v[116:119], v221 offset:16384\n\t" \
  "s_waitcnt lgkmcnt(8)\n\t" \
  "v_mfma_f32_16x16x32_bf16 v[152:155], v[204:207], v[104:107], v[152:155]\n\t" \
  "s_add_u32 %[sb], %[sb], 0x36000\n\t" \
  "s_cmp_eq_u32 %[sb], 0x1e6000\n\t" \
  "s_cselect_b32 %[sb], 0, %[sb]\n\t" \
  "s_add_u32 m0, %[m1], 67584\n\t" \
  "s_nop 0\n\t" \
  "buffer_load_dwordx4 v237, %[rb], %[sb] offen lds\n\t" \
  "s_cmp_lg_u32 %[m1], %[l0]\n\t" \
  "s_cbranch_scc1 .Lp1t16_%=\n\t" \
  "s_add_u32 m0, %[l0], 75776\n\t" \
  "s_nop 0\n\t" \
  "buffer_load_dwordx4 v238, %[rb], %[sb] offen lds\n\t" \
  ".Lp1t16_%=:\n\t" \
  "ds_read_b64_tr_b16 v[180:181], v225 offset:36864\n\t" \
  "v_mfma_f32_16x16x32_bf16 v[156:159], v[204:207], v[108:111], v[156:159]\n\t" \
  "ds_read_b64_tr_b16 v[182:183], v225 offset:41472\n\t" \
  "ds_read_b64_tr_b16 v[184:185], v225 offset:36872\n\t" \
  "s_waitcnt lgkmcnt(9)\n\t" \
  "v_mfma_f32_16x16x32_bf16 v[160:163], v[208:211], v[104:107], v[160:163]\n\t" \
  "ds_read_b64_tr_b16 v[186:187], v225 offset:41480\n\t" \
  "v_mfma_f32_16x16x32_bf16 v[164:167], v[208:211], v[108:111], v[164:167]\n\t" \
  "ds_read_b64_tr_b16 v[188:189], v225 offset:36936\n\t" \
  "ds_read_b64_tr_b16 v[190:191], v225 offset:41544\n\t" \
  "s_waitcnt lgkmcnt(10)\n\t" \
  "v_mfma_f32_16x16x32_bf16 v[168:171], v[212:215], v[104:107], v[168:171]\n\t" \
  "s_add_u32 %[sb], %[sb], 0x36000\n\t" \
  "s_cmp_eq_u32 %[sb], 0x1e6000\n\t" \
  "s_cselect_b32 %[sb], 0, %[sb]\n\t" \
  "s_add_u32 m0, %[m1], 76800\n\t" \
  "s_nop 0\n\t" \
  "buffer_load_dwordx4 v237, %[rb], %[sb] offen lds\n\t" \
  "s_cmp_lg_u32 %[m1], %[l0]\n\t" \
  "s_cbranch_scc1 .Lp1t17_%=\n\t" \
  "s_add_u32 m0, %[l0], 84992\n\t" \
  "s_nop 0\n\t" \
  "buffer_load_dwordx4 v238, %[rb], %[sb] offen lds\n\t" \
  ".Lp1t17_%=:\n\t" \
  "ds_read_b64_tr_b16 v[192:193], v225 offset:36944\n\t" \
  "ds_read_b64_tr_b16 v[194:195], v225 offset:41552\n\t" \
  "v_mfma_f32_16x16x32_bf16 v[172:175], v[212:215], v[108:111], v[172:175]\n\t" \
  "ds_read_b64_tr_b16 v[196:197], v227 offset:36864\n\t" \
  "s_waitcnt lgkmcnt(11)\n\t" \
  "v_mfma_f32_16x16x32_bf16 v[176:179], v[216:219], v[104:107], v[176:179]\n\t" \
  "ds_read_b64_tr_b16 v[198:199], v227 offset:41472\n\t" \
  "s_waitcnt lgkmcnt(8)\n\t" \
  "v_mfma_f32_16x16x32_bf16 v[144:147], v[180:183], v[112:115], v[144:147]\n\t" \
  "ds_read_b128 v[120:123], v222 offset:16384\n\t" \
  "v_mfma_f32_16x16x32_bf16 v[148:151], v[180:183], v[116:119], v[148:151]\n\t" \
  "ds_read_b128 v[124:127], v223 offset:16384\n\t" \
  "s_waitcnt lgkmcnt(8)\n\t" \
  "v_mfma_f32_16x16x32_bf16 v[152:155], v[184:187], v[112:115], v[152:155]\n\t" \
  "ds_read_b64_tr_b16 v[200:201], v225 offset:46080\n\t" \
  "v_mfma_f32_16x16x32_bf16 v[156:159], v[184:187], v[116:119], v[156:159]\n\t" \
  "ds_read_b64_tr_b16 v[202:203], v225 offset:50688\n\t" \
  "ds_read_b64_tr_b16 v[204:205], v225 offset:46088\n\t" \
  "s_waitcnt lgkmcnt(9)\n\t" \
  "v_mfma_f32_16x16x32_bf16 v[160:163], v[188:191], v[112:115], v[160:163]\n\t" \
  "s_add_u32 %[sb], %[sb], 0x36000\n\t" \
  "s_cmp_eq_u32 %[sb], 0x1e6000\n\t" \
  "s_cselect_b32 %[sb], 0, %[sb]\n\t" \
  "s_add_u32 m0, %[m1], 104448\n\t" \
  "s_nop 0\n\t" \
  "buffer_load_dwordx4 v237, %[rb], %[sb] offen lds\n\t" \
  "s_cmp_lg_u32 %[m1], %[l0]\n\t" \
  "s_cbranch_scc1 .Lp1t18_%=\n\t" \
  "s_add_u32 m0, %[l0], 112640\n\t" \
  "s_nop 0\n\t" \
  "buffer_load_dwordx4 v238, %[rb], %[sb] offen lds\n\t" \
  ".Lp1t18_%=:\n\t" \
  "ds_read_b64_tr_b16 v[206:207], v225 offset:50696\n\t" \
  "v_mfma_f32_16x16x32_bf16 v[164:167], v[188:191], v[116:119], v[164:167]\n\t" \
  "ds_read_b64_tr_b16 v[208:209], v225 offset:46152\n\t" \
  "ds_read_b64_tr_b16 v[210:211], v225 offset:50760\n\t" \
  "s_waitcnt lgkmcnt(10)\n\t" \
  "v_mfma_f32_16x16x32_bf16 v[168:171], v[192:195], v[112:115], v[168:171]\n\t" \
  "ds_read_b64_tr_b16 v[212:213], v225 offset:46160\n\t" \
  "ds_read_b64_tr_b16 v[214:215], v225 offset:50768\n\t" \
  "v_mfma_f32_16x16x32_bf16 v[172:175], v[192:195], v[116:119], v[172:175]\n\t" \
  "ds_read_b64_tr_b16 v[216:217], v227 offset:46080\n\t" \
  "s_waitcnt lgkmcnt(11)\n\t" \
  "v_mfma_f32_16x16x32_bf16 v[176:179], v[196:199], v[112:115], v[176:179]\n\t" \
  "ds_read_b64_tr_b16 v[218:219], v227 offset:50688\n\t" \
  "s_waitcnt vmcnt(5) lgkmcnt(10)\n\t" \
  "s_barrier\n\t" \
  "s_waitcnt lgkmcnt(8)\n\t" \
  "v_mfma_f32_16x16x32_bf16 v[144:147], v[200:203], v[120:123], v[144:147]\n\t" \
  "ds_read_b128 v[128:131], v220 offset:32768\n\t" \
  "v_mfma_f32_16x16x32_bf16 v[148:151], v[200:203], v[124:127], v[148:151]\n\t" \
  "ds_read_b128 v[132:135], v221 offset:32768\n\t" \
  "s_waitcnt lgkmcnt(8)\n\t" \
  "v_mfma_f32_16x16x32_bf16 v[152:155], v[204:207], v[120:123], v[152:155]\n\t" \
  "s_add_u32 %[sb], %[sb], 0x36000\n\t" \
  "s_cmp_eq_u32 %[sb], 0x1e6000\n\t" \
  "s_cselect_b32 %[sb], 0, %[sb]\n\t" \
  "s_add_u32 m0, %[m1], 113664\n\t" \
  "s_nop 0\n\t" \
  "buffer_load_dwordx4 v237, %[rb], %[sb] offen lds\n\t" \
  "s_cmp_lg_u32 %[m1], %[l0]\n\t" \
  "s_cbranch_scc1 .Lp1t19_%=\n\t" \
  "s_add_u32 m0, %[l0], 121856\n\t" \
  "s_nop 0\n\t" \
  "buffer_load_dwordx4 v238, %[rb], %[sb] offen lds\n\t" \
  ".Lp1t19_%=:\n\t" \
  "ds_read_b64_tr_b16 v[180:181], v224 offset:36864\n\t" \
  "v_mfma_f32_16x16x32_bf16 v[156:159], v[204:207], v[124:127], v[156:159]\n\t" \
  "ds_read_b64_tr_b16 v[182:183], v224 offset:41472\n\t" \
  "ds_read_b64_tr_b16 v[184:185], v224 offset:36872\n\t" \
  "s_waitcnt lgkmcnt(9)\n\t" \
  "v_mfma_f32_16x16x32_bf16 v[160:163], v[208:211], v[120:123], v[160:163]\n\t" \
  "ds_read_b64_tr_b16 v[186:187], v224 offset:41480\n\t" \
  "v_mfma_f32_16x16x32_bf16 v[164:167], v[208:211], v[124:127], v[164:167]\n\t" \
  "ds_read_b64_tr_b16 v[188:189], v224 offset:36936\n\t" \
  "ds_read_b64_tr_b16 v[190:191], v224 offset:41544\n\t" \
  "s_waitcnt lgkmcnt(10)\n\t" \
  "v_mfma_f32_16x16x32_bf16 v[168:171], v[212:215], v[120:123], v[168:171]\n\t" \
  "s_add_u32 %[sb], %[sb], 0x36000\n\t" \
  "s_cmp_eq_u32 %[sb], 0x1e6000\n\t" \
  "s_cselect_b32 %[sb], 0, %[sb]\n\t" \
  "s_add_u32 m0, %[m1], 122880\n\t" \
  "s_nop 0\n\t" \
  "buffer_load_dwordx4 v237, %[rb], %[sb] offen lds\n\t" \
  "s_cmp_lg_u32 %[m1], %[l0]\n\t" \
  "s_cbranch_scc1 .Lp1t20_%=\n\t" \
  "s_add_u32 m0, %[l0], 131072\n\t" \
  "s_nop 0\n\t" \
  "buffer_load_dwordx4 v238, %[rb], %[sb] offen lds\n\t" \
  ".Lp1t20_%=:\n\t" \
  "ds_read_b64_tr_b16 v[192:193], v224 offset:36944\n\t" \
  "ds_read_b64_tr_b16 v[194:195], v224 offset:41552\n\t" \
  "v_mfma_f32_16x16x32_bf16 v[172:175], v[212:215], v[124:127], v[172:175]\n\t" \
  "ds_read_b64_tr_b16 v[196:197], v226 offset:36864\n\t" \
  "s_waitcnt lgkmcnt(11)\n\t" \
  "v_mfma_f32_16x16x32_bf16 v[176:179], v[216:219], v[120:123], v[176:179]\n\t" \
  "ds_read_b64_tr_b16 v[198:199], v226 offset:41472\n\t" \
  "s_waitcnt lgkmcnt(8)\n\t" \
  "v_mfma_f32_16x16x32_bf16 v[144:147], v[180:183], v[128:131], v[144:147]\n\t" \
  "ds_read_b128 v[136:139], v222 offset:32768\n\t" \
  "v_mfma_f32_16x16x32_bf16 v[148:151], v[180:183], v[132:135], v[148:151]\n\t" \
  "ds_read_b128 v[140:143], v223 offset:32768\n\t" \
  "s_waitcnt lgkmcnt(8)\n\t" \
  "v_mfma_f32_16x16x32_bf16 v[152:155], v[184:187], v[128:131], v[152:155]\n\t" \
  "ds_read_b64_tr_b16 v[200:201], v224 offset:46080\n\t" \
  "v_mfma_f32_16x16x32_bf16 v[156:159], v[184:187], v[132:135], v[156:159]\n\t" \
  "ds_read_b64_tr_b16 v[202:203], v224 offset:50688\n\t" \
  "ds_read_b64_tr_b16 v[204:205], v224 offset:46088\n\t" \
  "s_waitcnt lgkmcnt(9)\n\t" \
  "v_mfma_f32_16x16x32_bf16 v[160:163], v[188:191], v[128:131], v[160:163]\n\t" \
  "s_add_u32 %[sb], %[sb], 0x36000\n\t" \
  "s_cmp_eq_u32 %[sb], 0x1e6000\n\t" \
  "s_cselect_b32 %[sb], 0, %[sb]\n\t" \
  "s_add_u32 m0, %[m1], 132096\n\t" \
  "s_nop 0\n\t" \
  "buffer_load_dwordx4 v237, %[rb], %[sb] offen lds\n\t" \
  "s_cmp_lg_u32 %[m1], %[l0]\n\t" \
  "s_cbranch_scc1 .Lp1t21_%=\n\t" \
  "s_add_u32 m0, %[l0], 140288\n\t" \
  "s_nop 0\n\t" \
  "buffer_load_dwordx4 v238, %[rb], %[sb] offen lds\n\t" \
  ".Lp1t21_%=:\n\t" \
  "ds_read_b64_tr_b16 v[206:207], v224 offset:50696\n\t" \
  "v_mfma_f32_16x16x32_bf16 v[164:167], v[188:191], v[132:135], v[164:167]\n\t" \
  "ds_read_b64_tr_b16 v[208:209], v224 offset:46152\n\t" \
  "ds_read_b64_tr_b16 v[210:211], v224 offset:50760\n\t" \
  "s_waitcnt lgkmcnt(10)\n\t" \
  "v_mfma_f32_16x16x32_bf16 v[168:171], v[192:195], v[128:131], v[168:171]\n\t" \
  "ds_read_b64_tr_b16 v[212:213], v224 offset:46160\n\t" \
  "ds_read_b64_tr_b16 v[214:215], v224 offset:50768\n\t" \
  "v_mfma_f32_16x16x32_bf16 v[172:175], v[192:195], v[132:135], v[172:175]\n\t" \
  "ds_read_b64_tr_b16 v[216:217], v226 offset:46080\n\t" \
  "s_waitcnt lgkmcnt(11)\n\t" \
  "v_mfma_f32_16x16x32_bf16 v[176:179], v[196:199], v[128:131], v[176:179]\n\t" \
  "ds_read_b64_tr_b16 v[218:219], v226 offset:50688\n\t" \
  "s_waitcnt vmcnt(4) lgkmcnt(10)\n\t" \
  "s_barrier\n\t" \
  "s_waitcnt lgkmcnt(8)\n\t" \
  "v_mfma_f32_16x16x32_bf16 v[144:147], v[200:203], v[136:139], v[144:147]\n\t" \
  "ds_read_b64_tr_b16 v[180:181], v228 offset:0\n\t" \
  "v_mfma_f32_16x16x32_bf16 v[148:151], v[200:203], v[140:143], v[148:151]\n\t" \
  "s_waitcnt lgkmcnt(7)\n\t" \
  "v_mfma_f32_16x16x32_bf16 v[152:155], v[204:207], v[136:139], v[152:155]\n\t" \
  "ds_read_b64_tr_b16 v[182:183], v228 offset:2304\n\t" \
  "v_mfma_f32_16x16x32_bf16 v[156:159], v[204:207], v[140:143], v[156:159]\n\t" \
  "ds_read_b64_tr_b16 v[184:185], v228 offset:8\n\t" \
  "s_waitcnt lgkmcnt(7)\n\t" \
  "v_mfma_f32_16x16x32_bf16 v[160:163], v[208:211], v[136:139], v[160:163]\n\t" \
  "ds_read_b64_tr_b16 v[186:187], v228 offset:2312\n\t" \
  "v_mfma_f32_16x16x32_bf16 v[164:167], v[208:211], v[140:143], v[164:167]\n\t" \
  "s_waitcnt lgkmcnt(6)\n\t" \
  "v_mfma_f32_16x16x32_bf16 v[168:171], v[212:215], v[136:139], v[168:171]\n\t" \
  "ds_read_b64_tr_b16 v[196:197], v230 offset:0\n\t" \
  "v_mfma_f32_16x16x32_bf16 v[172:175], v[212:215], v[140:143], v[172:175]\n\t" \
  "ds_read_b64_tr_b16 v[198:199], v230 offset:2304\n\t" \
  "s_waitcnt lgkmcnt(6)\n\t" \
  "v_mfma_f32_16x16x32_bf16 v[176:179], v[216:219], v[136:139], v[176:179]\n\t" \
  "s_waitcnt lgkmcnt(0)\n\t" \
  "s_nop 7\n\t"

#define P1B_T \
  "v_mfma_f32_16x16x32_bf16 v[144:147], v[180:183], v[0:3], 0\n\t" \
  "ds_read_b64_tr_b16 v[200:201], v228 offset:4608\n\t" \
  "v_mfma_f32_16x16x32_bf16 v[148:151], v[180:183], v[4:7], 0\n\t" \
  "ds_read_b64_tr_b16 v[202:203], v228 offset:6912\n\t" \
  "ds_read_b64_tr_b16 v[204:205], v228 offset:4616\n\t" \
  "v_mfma_f32_16x16x32_bf16 v[152:155], v[184:187], v[0:3], 0\n\t" \
  "ds_read_b64_tr_b16 v[206:207], v228 offset:6920\n\t" \
  "v_mfma_f32_16x16x32_bf16 v[156:159], v[184:187], v[4:7], 0\n\t" \
  "ds_read_b64_tr_b16 v[216:217], v230 offset:4608\n\t" \
  "v_mfma_f32_16x16x32_bf16 v[176:179], v[196:199], v[0:3], 0\n\t" \
  "ds_read_b64_tr_b16 v[218:219], v230 offset:6912\n\t" \
  "s_waitcnt lgkmcnt(4)\n\t" \
  "v_mfma_f32_16x16x32_bf16 v[144:147], v[200:203], v[8:11], v[144:147]\n\t" \
  "ds_read_b64_tr_b16 v[180:181], v228 offset:9216\n\t" \
  "v_mfma_f32_16x16x32_bf16 v[148:151], v[200:203], v[12:15], v[148:151]\n\t" \
  "ds_read_b64_tr_b16 v[182:183], v228 offset:11520\n\t" \
  "ds_read_b64_tr_b16 v[184:185], v228 offset:9224\n\t" \
  "s_waitcnt lgkmcnt(5)\n\t" \
  "v_mfma_f32_16x16x32_bf16 v[152:155], v[204:207], v[8:11], v[152:155]\n\t" \
  "ds_read_b64_tr_b16 v[186:187], v228 offset:11528\n\t" \
  "v_mfma_f32_16x16x32_bf16 v[156:159], v[204:207], v[12:15], v[156:159]\n\t" \
  "ds_read_b64_tr_b16 v[196:197], v230 offset:9216\n\t" \
  "s_waitcnt lgkmcnt(5)\n\t" \
  "v_mfma_f32_16x16x32_bf16 v[176:179], v[216:219], v[8:11], v[176:179]\n\t" \
  "ds_read_b64_tr_b16 v[198:199], v230 offset:11520\n\t" \
  "s_waitcnt lgkmcnt(4)\n\t" \
  "v_mfma_f32_16x16x32_bf16 v[144:147], v[180:183], v[16:19], v[144:147]\n\t" \
  "ds_read_b64_tr_b16 v[200:201], v228 offset:13824\n\t" \
  "v_mfma_f32_16x16x32_bf16 v[148:151], v[180:183], v[20:23], v[148:151]\n\t" \
  "ds_read_b64_tr_b16 v[202:203], v228 offset:16128\n\t" \
  "ds_read_b64_tr_b16 v[204:205], v228 offset:13832\n\t" \
  "s_waitcnt lgkmcnt(5)\n\t" \
  "v_mfma_f32_16x16x32_bf16 v[152:155], v[184:187], v[16:19], v[152:155]\n\t" \
  "ds_read_b64_tr_b16 v[206:207], v228 offset:16136\n\t" \
  "v_mfma_f32_16x16x32_bf16 v[156:159], v[184:187], v[20:23], v[156:159]\n\t" \
  "ds_read_b64_tr_b16 v[216:217], v230 offset:13824\n\t" \
  "s_waitcnt lgkmcnt(5)\n\t" \
  "v_mfma_f32_16x16x32_bf16 v[176:179], v[196:199], v[16:19], v[176:179]\n\t" \
  "ds_read_b64_tr_b16 v[218:219], v230 offset:16128\n\t" \
  "s_waitcnt lgkmcnt(4)\n\t" \
  "v_mfma_f32_16x16x32_bf16 v[144:147], v[200:203], v[24:27], v[144:147]\n\t" \
  "ds_read_b64_tr_b16 v[180:181], v228 offset:18432\n\t" \
  "v_mfma_f32_16x16x32_bf16 v[148:151], v[200:203], v[28:31], v[148:151]\n\t" \
  "ds_read_b64_tr_b16 v[182:183], v228 offset:20736\n\t" \
  "ds_read_b64_tr_b16 v[184:185], v228 offset:18440\n\t" \
  "s_waitcnt lgkmcnt(5)\n\t" \
  "v_mfma_f32_16x16x32_bf16 v[152:155], v[204:207], v[24:27], v[152:155]\n\t" \
  "ds_read_b64_tr_b16 v[186:187], v228 offset:20744\n\t" \
  "v_mfma_f32_16x16x32_bf16 v[156:159], v[204:207], v[28:31], v[156:159]\n\t" \
  "ds_read_b64_tr_b16 v[196:197], v230 offset:18432\n\t" \
  "s_waitcnt lgkmcnt(5)\n\t" \
  "v_mfma_f32_16x16x32_bf16 v[176:179], v[216:219], v[24:27], v[176:179]\n\t" \
  "ds_read_b64_tr_b16 v[198:199], v230 offset:20736\n\t" \
  "s_waitcnt lgkmcnt(4)\n\t" \
  "v_mfma_f32_16x16x32_bf16 v[144:147], v[180:183], v[32:35], v[144:147]\n\t" \
  "ds_read_b64_tr_b16 v[200:201], v228 offset:23040\n\t" \
  "v_mfma_f32_16x16x32_bf16 v[148:151], v[180:183], v[36:39], v[148:151]\n\t" \
  "ds_read_b64_tr_b16 v[202:203], v228 offset:25344\n\t" \
  "ds_read_b64_tr_b16 v[204:205], v228 offset:23048\n\t" \
  "s_waitcnt lgkmcnt(5)\n\t" \
  "v_mfma_f32_16x16x32_bf16 v[152:155], v[184:187], v[32:35], v[152:155]\n\t" \
  "ds_read_b64_tr_b16 v[206:207], v228 offset:25352\n\t" \
  "v_mfma_f32_16x16x32_bf16 v[156:159], v[184:187], v[36:39], v[156:159]\n\t" \
  "ds_read_b64_tr_b16 v[216:217], v230 offset:23040\n\t" \
  "s_waitcnt lgkmcnt(5)\n\t" \
  "v_mfma_f32_16x16x32_bf16 v[176:179], v[196:199], v[32:35], v[176:179]\n\t" \
  "ds_read_b64_tr_b16 v[218:219], v230 offset:25344\n\t" \
  "s_waitcnt lgkmcnt(4)\n\t" \
  "v_mfma_f32_16x16x32_bf16 v[144:147], v[200:203], v[40:43], v[144:147]\n\t" \
  "ds_read_b64_tr_b16 v[180:181], v228 offset:27648\n\t" \
  "v_mfma_f32_16x16x32_bf16 v[148:151], v[200:203], v[44:47], v[148:151]\n\t" \
  "ds_read_b64_tr_b16 v[182:183], v228 offset:29952\n\t" \
  "ds_read_b64_tr_b16 v[184:185], v228 offset:27656\n\t" \
  "s_waitcnt lgkmcnt(5)\n\t" \
  "v_mfma_f32_16x16x32_bf16 v[152:155], v[204:207], v[40:43], v[152:155]\n\t" \
  "ds_read_b64_tr_b16 v[186:187], v228 offset:29960\n\t" \
  "v_mfma_f32_16x16x32_bf16 v[156:159], v[204:207], v[44:47], v[156:159]\n\t" \
  "ds_read_b64_tr_b16 v[196:197], v230 offset:27648\n\t" \
  "s_waitcnt lgkmcnt(5)\n\t" \
  "v_mfma_f32_16x16x32_bf16 v[176:179], v[216:219], v[40:43], v[176:179]\n\t" \
  "ds_read_b64_tr_b16 v[198:199], v230 offset:29952\n\t" \
  "s_waitcnt lgkmcnt(4)\n\t" \
  "v_mfma_f32_16x16x32_bf16 v[144:147], v[180:183], v[48:51], v[144:147]\n\t" \
  "ds_read_b64_tr_b16 v[200:201], v228 offset:32256\n\t" \
  "v_mfma_f32_16x16x32_bf16 v[148:151], v[180:183], v[52:55], v[148:151]\n\t" \
  "ds_read_b64_tr_b16 v[202:203], v228 offset:34560\n\t" \
  "ds_read_b64_tr_b16 v[204:205], v228 offset:32264\n\t" \
  "s_waitcnt lgkmcnt(5)\n\t" \
  "v_mfma_f32_16x16x32_bf16 v[152:155], v[184:187], v[48:51], v[152:155]\n\t" \
  "ds_read_b64_tr_b16 v[206:207], v228 offset:34568\n\t" \
  "v_mfma_f32_16x16x32_bf16 v[156:159], v[184:187], v[52:55], v[156:159]\n\t" \
  "ds_read_b64_tr_b16 v[216:217], v230 offset:32256\n\t" \
  "s_waitcnt lgkmcnt(5)\n\t" \
  "v_mfma_f32_16x16x32_bf16 v[176:179], v[196:199], v[48:51], v[176:179]\n\t" \
  "ds_read_b64_tr_b16 v[218:219], v230 offset:34560\n\t" \
  "s_waitcnt vmcnt(0)\n\t" \
  "s_barrier\n\t" \
  "s_cmp_lg_u32 %[done], 2\n\t" \
  "s_cbranch_scc1 .Lp1t22_%=\n\t" \
  "global_store_dwordx2 %[vz], %[tag], %[fl] sc1\n\t" \
  ".Lp1t22_%=:\n\t" \
  "s_add_u32 %[sb], %[sb], 0x36000\n\t" \
  "s_cmp_eq_u32 %[sb], 0x1e6000\n\t" \
  "s_cselect_b32 %[sb], 0, %[sb]\n\t" \
  "s_add_u32 m0, %[m1], 49152\n\t" \
  "s_nop 0\n\t" \
  "buffer_load_dwordx4 v237, %[rb], %[sb] offen lds\n\t" \
  "s_cmp_lg_u32 %[m1], %[l0]\n\t" \
  "s_cbranch_scc1 .Lp1t23_%=\n\t" \
  "s_add_u32 m0, %[l0], 57344\n\t" \
  "s_nop 0\n\t" \
  "buffer_load_dwordx4 v238, %[rb], %[sb] offen lds\n\t" \
  ".Lp1t23_%=:\n\t" \
  "s_waitcnt lgkmcnt(4)\n\t" \
  "v_mfma_f32_16x16x32_bf16 v[144:147], v[200:203], v[56:59], v[144:147]\n\t" \
  "ds_read_b64_tr_b16 v[180:181], v229 offset:0\n\t" \
  "v_mfma_f32_16x16x32_bf16 v[148:151], v[200:203], v[60:63], v[148:151]\n\t" \
  "ds_read_b64_tr_b16 v[182:183], v229 offset:2304\n\t" \
  "ds_read_b64_tr_b16 v[184:185], v229 offset:8\n\t" \
  "s_waitcnt lgkmcnt(5)\n\t" \
  "v_mfma_f32_16x16x32_bf16 v[152:155], v[204:207], v[56:59], v[152:155]\n\t" \
  "ds_read_b64_tr_b16 v[186:187], v229 offset:2312\n\t" \
  "v_mfma_f32_16x16x32_bf16 v[156:159], v[204:207], v[60:63], v[156:159]\n\t" \
  "ds_read_b64_tr_b16 v[196:197], v231 offset:0\n\t" \
  "s_waitcnt lgkmcnt(5)\n\t" \
  "v_mfma_f32_16x16x32_bf16 v[176:179], v[216:219], v[56:59], v[176:179]\n\t" \
  "ds_read_b64_tr_b16 v[198:199], v231 offset:2304\n\t" \
  "s_waitcnt lgkmcnt(4)\n\t" \
  "v_mfma_f32_16x16x32_bf16 v[144:147], v[180:183], v[64:67], v[144:147]\n\t" \
  "ds_read_b64_tr_b16 v[200:201], v229 offset:4608\n\t" \
  "v_mfma_f32_16x16x32_bf16 v[148:151], v[180:183], v[68:71], v[148:151]\n\t" \
  "ds_read_b64_tr_b16 v[202:203], v229 offset:6912\n\t" \
  "ds_read_b64_tr_b16 v[204:205], v229 offset:4616\n\t" \
  "s_waitcnt lgkmcnt(5)\n\t" \
  "v_mfma_f32_16x16x32_bf16 v[152:155], v[184:187], v[64:67], v[152:155]\n\t" \
  "ds_read_b64_tr_b16 v[206:207], v229 offset:6920\n\t" \
  "v_mfma_f32_16x16x32_bf16 v[156:159], v[184:187], v[68:71], v[156:159]\n\t" \
  "ds_read_b64_tr_b16 v[216:217], v231 offset:4608\n\t" \
  "s_waitcnt lgkmcnt(5)\n\t" \
  "v_mfma_f32_16x16x32_bf16 v[176:179], v[196:199], v[64:67], v[176:179]\n\t" \
  "ds_read_b64_tr_b16 v[218:219], v231 offset:6912\n\t" \
  "s_waitcnt lgkmcnt(4)\n\t" \
  "v_mfma_f32_16x16x32_bf16 v[144:147], v[200:203], v[72:75], v[144:147]\n\t" \
  "ds_read_b64_tr_b16 v[180:181], v229 offset:9216\n\t" \
  "v_mfma_f32_16x16x32_bf16 v[148:151], v[200:203], v[76:79], v[148:151]\n\t" \
  "ds_read_b64_tr_b16 v[182:183], v229 offset:11520\n\t" \
  "ds_read_b64_tr_b16 v[184:185], v229 offset:9224\n\t" \
  "s_waitcnt lgkmcnt(5)\n\t" \
  "v_mfma_f32_16x16x32_bf16 v[152:155], v[204:207], v[72:75], v[152:155]\n\t" \
  "ds_read_b64_tr_b16 v[186:187], v229 offset:11528\n\t" \
  "v_mfma_f32_16x16x32_bf16 v[156:159], v[204:207], v[76:79], v[156:159]\n\t" \
  "ds_read_b64_tr_b16 v[196:197], v231 offset:9216\n\t" \
  "s_waitcnt lgkmcnt(5)\n\t" \
  "v_mfma_f32_16x16x32_bf16 v[176:179], v[216:219], v[72:75], v[176:179]\n\t" \
  "ds_read_b64_tr_b16 v[198:199], v231 offset:11520\n\t" \
  "s_waitcnt lgkmcnt(4)\n\t" \
  "v_mfma_f32_16x16x32_bf16 v[144:147], v[180:183], v[80:83], v[144:147]\n\t" \
  "ds_read_b64_tr_b16 v[200:201], v229 offset:13824\n\t" \
  "v_mfma_f32_16x16x32_bf16 v[148:151], v[180:183], v[84:87], v[148:151]\n\t" \
  "ds_read_b64_tr_b16 v[202:203], v229 offset:16128\n\t" \
  "ds_read_b64_tr_b16 v[204:205], v229 offset:13832\n\t" \
  "s_waitcnt lgkmcnt(5)\n\t" \
  "v_mfma_f32_16x16x32_bf16 v[152:155], v[184:187], v[80:83], v[152:155]\n\t" \
  "ds_read_b64_tr_b16 v[206:207], v229 offset:16136\n\t" \
  "v_mfma_f32_16x16x32_bf16 v[156:159], v[184:187], v[84:87], v[156:159]\n\t" \
  "ds_read_b64_tr_b16 v[216:217], v231 offset:13824\n\t" \
  "s_waitcnt lgkmcnt(5)\n\t" \
  "v_mfma_f32_16x16x32_bf16 v[176:179], v[196:199], v[80:83], v[176:179]\n\t" \
  "ds_read_b64_tr_b16 v[218:219], v231 offset:16128\n\t" \
  "s_waitcnt lgkmcnt(4)\n\t" \
  "v_mfma_f32_16x16x32_bf16 v[144:147], v[200:203], v[88:91], v[144:147]\n\t" \
  "ds_read_b64_tr_b16 v[180:181], v229 offset:18432\n\t" \
  "v_mfma_f32_16x16x32_bf16 v[148:151], v[200:203], v[92:95], v[148:151]\n\t" \
  "ds_read_b64_tr_b16 v[182:183], v229 offset:20736\n\t" \
  "ds_read_b64_tr_b16 v[184:185], v229 offset:18440\n\t" \
  "s_waitcnt lgkmcnt(5)\n\t" \
  "v_mfma_f32_16x16x32_bf16 v[152:155], v[204:207], v[88:91], v[152:155]\n\t" \
  "ds_read_b64_tr_b16 v[186:187], v229 offset:20744\n\t" \
  "v_mfma_f32_16x16x32_bf16 v[156:159], v[204:207], v[92:95], v[156:159]\n\t" \
  "ds_read_b64_tr_b16 v[196:197], v231 offset:18432\n\t" \
  "s_waitcnt lgkmcnt(5)\n\t" \
  "v_mfma_f32_16x16x32_bf16 v[176:179], v[216:219], v[88:91], v[176:179]\n\t" \
  "ds_read_b64_tr_b16 v[198:199], v231 offset:20736\n\t" \
  "s_waitcnt lgkmcnt(4)\n\t" \
  "v_mfma_f32_16x16x32_bf16 v[144:147], v[180:183], v[96:99], v[144:147]\n\t" \
  "ds_read_b64_tr_b16 v[200:201], v229 offset:23040\n\t" \
  "v_mfma_f32_16x16x32_bf16 v[148:151], v[180:183], v[100:103], v[148:151]\n\t" \
  "ds_read_b64_tr_b16 v[202:203], v229 offset:25344\n\t" \
  "ds_read_b64_tr_b16 v[204:205], v229 offset:23048\n\t" \
  "s_waitcnt lgkmcnt(5)\n\t" \
  "v_mfma_f32_16x16x32_bf16 v[152:155], v[184:187], v[96:99], v[152:155]\n\t" \
  "ds_read_b64_tr_b16 v[206:207], v229 offset:25352\n\t" \
  "v_mfma_f32_16x16x32_bf16 v[156:159], v[184:187], v[100:103], v[156:159]\n\t" \
  "ds_read_b64_tr_b16 v[216:217], v231 offset:23040\n\t" \
  "s_waitcnt lgkmcnt(5)\n\t" \
  "v_mfma_f32_16x16x32_bf16 v[176:179], v[196:199], v[96:99], v[176:179]\n\t" \
  "ds_read_b64_tr_b16 v[218:219], v231 offset:25344\n\t" \
  "s_waitcnt lgkmcnt(4)\n\t" \
  "v_mfma_f32_16x16x32_bf16 v[144:147], v[200:203], v[104:107], v[144:147]\n\t" \
  "ds_read_b64_tr_b16 v[180:181], v229 offset:27648\n\t" \
  "v_mfma_f32_16x16x32_bf16 v[148:151], v[200:203], v[108:111], v[148:151]\n\t" \
  "ds_read_b64_tr_b16 v[182:183], v229 offset:29952\n\t" \
  "ds_read_b64_tr_b16 v[184:185], v229 offset:27656\n\t" \
  "s_waitcnt lgkmcnt(5)\n\t" \
  "v_mfma_f32_16x16x32_bf16 v[152:155], v[204:207], v[104:107], v[152:155]\n\t" \
  "ds_read_b64_tr_b16 v[186:187], v229 offset:29960\n\t" \
  "v_mfma_f32_16x16x32_bf16 v[156:159], v[204:207], v[108:111], v[156:159]\n\t" \
  "ds_read_b64_tr_b16 v[196:197], v231 offset:27648\n\t" \
  "s_waitcnt lgkmcnt(5)\n\t" \
  "v_mfma_f32_16x16x32_bf16 v[176:179], v[216:219], v[104:107], v[176:179]\n\t" \
  "ds_read_b64_tr_b16 v[198:199], v231 offset:29952\n\t" \
  "s_waitcnt vmcnt(0) lgkmcnt(6)\n\t" \
  "s_barrier\n\t" \
  "s_waitcnt lgkmcnt(4)\n\t" \
  "v_mfma_f32_16x16x32_bf16 v[144:147], v[180:183], v[112:115], v[144:147]\n\t" \
  "ds_read_b64_tr_b16 v[200:201], v229 offset:32256\n\t" \
  "v_mfma_f32_16x16x32_bf16 v[148:151], v[180:183], v[116:119], v[148:151]\n\t" \
  "ds_read_b64_tr_b16 v[202:203], v229 offset:34560\n\t" \
  "ds_read_b64_tr_b16 v[204:205], v229 offset:32264\n\t" \
  "s_waitcnt lgkmcnt(5)\n\t" \
  "v_mfma_f32_16x16x32_bf16 v[152:155], v[184:187], v[112:115], v[152:155]\n\t" \
  "ds_read_b64_tr_b16 v[206:207], v229 offset:34568\n\t" \
  "v_mfma_f32_16x16x32_bf16 v[156:159], v[184:187], v[116:119], v[156:159]\n\t" \
  "ds_read_b64_tr_b16 v[216:217], v231 offset:32256\n\t" \
  "s_waitcnt lgkmcnt(5)\n\t" \
  "v_mfma_f32_16x16x32_bf16 v[176:179], v[196:199], v[112:115], v[176:179]\n\t" \
  "ds_read_b64_tr_b16 v[218:219], v231 offset:34560\n\t" \
  "s_cmp_lg_u32 %[done], 0\n\t" \
  "s_cbranch_scc1 .Lp1t24_%=\n\t" \
  "global_load_dwordx2 %[pk], %[fa], %[fl] sc1\n\t" \
  ".Lp1t24_%=:\n\t" \
  "s_waitcnt lgkmcnt(4)\n\t" \
  "v_mfma_f32_16x16x32_bf16 v[144:147], v[200:203], v[120:123], v[144:147]\n\t" \
  "ds_read_b64_tr_b16 v[180:181], v228 offset:0\n\t" \
  "v_mfma_f32_16x16x32_bf16 v[148:151], v[200:203], v[124:127], v[148:151]\n\t" \
  "ds_read_b64_tr_b16 v[182:183], v228 offset:2304\n\t" \
  "ds_read_b64_tr_b16 v[184:185], v228 offset:8\n\t" \
  "s_waitcnt lgkmcnt(5)\n\t" \
  "v_mfma_f32_16x16x32_bf16 v[152:155], v[204:207], v[120:123], v[152:155]\n\t" \
  "ds_read_b64_tr_b16 v[186:187], v228 offset:2312\n\t" \
  "v_mfma_f32_16x16x32_bf16 v[156:159], v[204:207], v[124:127], v[156:159]\n\t" \
  "ds_read_b64_tr_b16 v[196:197], v230 offset:0\n\t" \
  "s_waitcnt lgkmcnt(5)\n\t" \
  "v_mfma_f32_16x16x32_bf16 v[176:179], v[216:219], v[120:123], v[176:179]\n\t" \
  "ds_read_b64_tr_b16 v[198:199], v230 offset:2304\n\t" \
  "s_waitcnt lgkmcnt(4)\n\t" \
  "v_mfma_f32_16x16x32_bf16 v[144:147], v[180:183], v[128:131], v[144:147]\n\t" \
  "ds_read_b64_tr_b16 v[200:201], v228 offset:4608\n\t" \
  "v_mfma_f32_16x16x32_bf16 v[148:151], v[180:183], v[132:135], v[148:151]\n\t" \
  "ds_read_b64_tr_b16 v[202:203], v228 offset:6912\n\t" \
  "ds_read_b64_tr_b16 v[204:205], v228 offset:4616\n\t" \
  "s_waitcnt lgkmcnt(5)\n\t" \
  "v_mfma_f32_16x16x32_bf16 v[152:155], v[184:187], v[128:131], v[152:155]\n\t" \
  "ds_read_b64_tr_b16 v[206:207], v228 offset:6920\n\t" \
  "v_mfma_f32_16x16x32_bf16 v[156:159], v[184:187], v[132:135], v[156:159]\n\t" \
  "ds_read_b64_tr_b16 v[216:217], v230 offset:4608\n\t" \
  "s_waitcnt lgkmcnt(5)\n\t" \
  "v_mfma_f32_16x16x32_bf16 v[176:179], v[196:199], v[128:131], v[176:179]\n\t" \
  "ds_read_b64_tr_b16 v[218:219], v230 offset:6912\n\t" \
  "s_waitcnt lgkmcnt(4)\n\t" \
  "v_mfma_f32_16x16x32_bf16 v[144:147], v[200:203], v[136:139], v[144:147]\n\t" \
  "v_mfma_f32_16x16x32_bf16 v[148:151], v[200:203], v[140:143], v[148:151]\n\t" \
  "s_waitcnt lgkmcnt(2)\n\t" \
  "v_mfma_f32_16x16x32_bf16 v[152:155], v[204:207], v[136:139], v[152:155]\n\t" \
  "v_mfma_f32_16x16x32_bf16 v[156:159], v[204:207], v[140:143], v[156:159]\n\t" \
  "s_waitcnt lgkmcnt(0)\n\t" \
  "v_mfma_f32_16x16x32_bf16 v[176:179], v[216:219], v[136:139], v[176:179]\n\t" \
  "s_waitcnt lgkmcnt(0)\n\t" \
  "s_nop 7\n\t" \
  "s_cmp_lg_u32 %[done], 0\n\t" \
  "s_cbranch_scc1 .Lp1t26_%=\n\t" \
  "s_waitcnt vmcnt(0)\n\t" \
  "v_cmp_ne_u64 vcc, %[pk], %[tag]\n\t" \
  "s_cbranch_vccnz .Lp1t25_%=\n\t" \
  "s_mov_b32 %[done], 1\n\t" \
  "s_add_u32 m0, %[m1], 150528\n\t" \
  "s_add_u32 %[sw], %[sq], 0\n\t" \
  "buffer_load_dwordx4 %[l16], %[rw], %[sw] offen sc1 lds\n\t" \
  "s_mov_b32 m0, %[m7]\n\t" \
  "s_nop 0\n\t" \
  "buffer_load_dwordx4 %[l16], %[rw], %[s7] offen sc1 lds\n\t" \
  "s_add_u32 m0, %[m1], 0\n\t" \
  "s_add_u32 %[sw], %[sk], 0\n\t" \
  "buffer_load_dwordx4 %[l16], %[rw], %[sw] offen sc1 lds\n\t" \
  "s_add_u32 m0, %[m1], 7168\n\t" \
  "s_add_u32 %[sw], %[sk], 7168\n\t" \
  "buffer_load_dwordx4 %[l16], %[rw], %[sw] offen sc1 lds\n\t" \
  "s_add_u32 m0, %[m1], 14336\n\t" \
  "s_add_u32 %[sw], %[sk], 14336\n\t" \
  "buffer_load_dwordx4 %[l16], %[rw], %[sw] offen sc1 lds\n\t" \
  "s_add_u32 m0, %[m1], 21504\n\t" \
  "s_add_u32 %[sw], %[sk], 21504\n\t" \
  "buffer_load_dwordx4 %[l16], %[rw], %[sw] offen sc1 lds\n\t" \
  "s_add_u32 m0, %[m1], 28672\n\t" \
  "s_add_u32 %[sw], %[sk], 28672\n\t" \
  "buffer_load_dwordx4 %[l16], %[rw], %[sw] offen sc1 lds\n\t" \
  "s_add_u32 m0, %[m1], 35840\n\t" \
  "s_add_u32 %[sw], %[sk], 35840\n\t" \
  "buffer_load_dwordx4 %[l16], %[rw], %[sw] offen sc1 lds\n\t" \
  "s_branch .Lp1t26_%=\n\t" \
  ".Lp1t25_%=:\n\t" \
  ".Lp1t26_%=:\n\t"

#define P1A_F \
  "s_waitcnt vmcnt(3) lgkmcnt(0)\n\t" \
  "s_barrier\n\t" \
  "ds_read_b128 v[0:3], v220 offset:0\n\t" \
  "ds_read_b128 v[4:7], v221 offset:0\n\t" \
  "ds_read_b64_tr_b16 v[180:181], v229 offset:18432\n\t" \
  "ds_read_b64_tr_b16 v[182:183], v229 offset:20736\n\t" \
  "ds_read_b64_tr_b16 v[184:185], v229 offset:18472\n\t" \
  "ds_read_b64_tr_b16 v[186:187], v229 offset:20776\n\t" \
  "ds_read_b64_tr_b16 v[196:197], v231 offset:18432\n\t" \
  "ds_read_b64_tr_b16 v[198:199], v231 offset:20736\n\t" \
  "s_waitcnt lgkmcnt(4)\n\t" \
  "v_mfma_f32_16x16x32_bf16 v[112:115], v[0:3], v[180:183], 0\n\t" \
  "ds_read_b128 v[8:11], v222 offset:0\n\t" \
  "v_mfma_f32_16x16x32_bf16 v[116:119], v[4:7], v[180:183], 0\n\t" \
  "s_add_u32 %[sx], %[sx], 128\n\t" \
  "s_cmp_eq_u32 %[sx], 0x480\n\t" \
  "s_cselect_b32 %[sx], 0, %[sx]\n\t" \
  "s_mul_i32 %[sw], %[sx], 0x6c0\n\t" \
  "s_add_u32 m0, %[m2], 32768\n\t" \
  "s_nop 0\n\t" \
  "buffer_load_dwordx4 v232, %[rx], %[sx] offen lds\n\t" \
  "ds_read_b128 v[12:15], v223 offset:0\n\t" \
  "s_waitcnt lgkmcnt(4)\n\t" \
  "v_mfma_f32_16x16x32_bf16 v[120:123], v[0:3], v[184:187], 0\n\t" \
  "ds_read_b64_tr_b16 v[200:201], v229 offset:23040\n\t" \
  "ds_read_b64_tr_b16 v[202:203], v229 offset:25344\n\t" \
  "v_mfma_f32_16x16x32_bf16 v[124:127], v[4:7], v[184:187], 0\n\t" \
  "s_add_u32 m0, %[m1], 141312\n\t" \
  "s_nop 0\n\t" \
  "buffer_load_dwordx4 v237, %[ra], %[sw] offen lds\n\t" \
  "s_cmp_lg_u32 %[m1], %[l0]\n\t" \
  "s_cbranch_scc1 .Lp1f3_%=\n\t" \
  "s_add_u32 m0, %[l0], 149504\n\t" \
  "s_nop 0\n\t" \
  "buffer_load_dwordx4 v238, %[ra], %[sw] offen lds\n\t" \
  ".Lp1f3_%=:\n\t" \
  "ds_read_b64_tr_b16 v[204:205], v229 offset:23080\n\t" \
  "s_waitcnt lgkmcnt(5)\n\t" \
  "s_cmp_eq_u32 %[ch], 0\n\t" \
  "s_cbranch_scc1 .Lp1f4_%=\n\t" \
  "v_mfma_f32_16x16x32_bf16 v[128:131], v[0:3], v[196:199], 0\n\t" \
  ".Lp1f4_%=:\n\t" \
  "ds_read_b64_tr_b16 v[206:207], v229 offset:25384\n\t" \
  "ds_read_b64_tr_b16 v[216:217], v231 offset:23040\n\t" \
  "s_cmp_eq_u32 %[ch], 0\n\t" \
  "s_cbranch_scc1 .Lp1f5_%=\n\t" \
  "v_mfma_f32_16x16x32_bf16 v[132:135], v[4:7], v[196:199], 0\n\t" \
  ".Lp1f5_%=:\n\t" \
  "s_add_u32 m0, %[m2], 33792\n\t" \
  "s_nop 0\n\t" \
  "buffer_load_dwordx4 v233, %[rx], %[sx] offen lds\n\t" \
  "ds_read_b64_tr_b16 v[218:219], v231 offset:25344\n\t" \
  "s_waitcnt vmcnt(3) lgkmcnt(6)\n\t" \
  "s_barrier\n\t" \
  "s_waitcnt lgkmcnt(4)\n\t" \
  "v_mfma_f32_16x16x32_bf16 v[112:115], v[8:11], v[200:203], v[112:115]\n\t" \
  "ds_read_b128 v[16:19], v220 offset:16384\n\t" \
  "v_mfma_f32_16x16x32_bf16 v[116:119], v[12:15], v[200:203], v[116:119]\n\t" \
  "s_add_u32 %[sx], %[sx], 128\n\t" \
  "s_cmp_eq_u32 %[sx], 0x480\n\t" \
  "s_cselect_b32 %[sx], 0, %[sx]\n\t" \
  "s_mul_i32 %[sw], %[sx], 0x6c0\n\t" \
  "s_add_u32 m0, %[m2], 0\n\t" \
  "s_nop 0\n\t" \
  "buffer_load_dwordx4 v232, %[rx], %[sx] offen lds\n\t" \
  "ds_read_b128 v[20:23], v221 offset:16384\n\t" \
  "s_waitcnt lgkmcnt(4)\n\t" \
  "v_mfma_f32_16x16x32_bf16 v[120:123], v[8:11], v[204:207], v[120:123]\n\t" \
  "ds_read_b64_tr_b16 v[180:181], v229 offset:27648\n\t" \
  "ds_read_b64_tr_b16 v[182:183], v229 offset:29952\n\t" \
  "v_mfma_f32_16x16x32_bf16 v[124:127], v[12:15], v[204:207], v[124:127]\n\t" \
  "ds_read_b64_tr_b16 v[184:185], v229 offset:27688\n\t" \
  "s_waitcnt lgkmcnt(5)\n\t" \
  "s_cmp_eq_u32 %[ch], 0\n\t" \
  "s_cbranch_scc1 .Lp1f6_%=\n\t" \
  "v_mfma_f32_16x16x32_bf16 v[128:131], v[8:11], v[216:219], v[128:131]\n\t" \
  ".Lp1f6_%=:\n\t" \
  "s_add_u32 m0, %[m1], 150528\n\t" \
  "s_nop 0\n\t" \
  "buffer_load_dwordx4 v237, %[ra], %[sw] offen lds\n\t" \
  "s_cmp_lg_u32 %[m1], %[l0]\n\t" \
  "s_cbranch_scc1 .Lp1f7_%=\n\t" \
  "s_add_u32 m0, %[l0], 158720\n\t" \
  "s_nop 0\n\t" \
  "buffer_load_dwordx4 v238, %[ra], %[sw] offen lds\n\t" \
  ".Lp1f7_%=:\n\t" \
  "ds_read_b64_tr_b16 v[186:187], v229 offset:29992\n\t" \
  "ds_read_b64_tr_b16 v[196:197], v231 offset:27648\n\t" \
  "s_cmp_eq_u32 %[ch], 0\n\t" \
  "s_cbranch_scc1 .Lp1f8_%=\n\t" \
  "v_mfma_f32_16x16x32_bf16 v[132:135], v[12:15], v[216:219], v[132:135]\n\t" \
  ".Lp1f8_%=:\n\t" \
  "ds_read_b64_tr_b16 v[198:199], v231 offset:29952\n\t" \
  "s_waitcnt lgkmcnt(4)\n\t" \
  "v_mfma_f32_16x16x32_bf16 v[112:115], v[16:19], v[180:183], v[112:115]\n\t" \
  "ds_read_b128 v[24:27], v222 offset:16384\n\t" \
  "v_mfma_f32_16x16x32_bf16 v[116:119], v[20:23], v[180:183], v[116:119]\n\t" \
  "ds_read_b128 v[28:31], v223 offset:16384\n\t" \
  "s_waitcnt lgkmcnt(4)\n\t" \
  "v_mfma_f32_16x16x32_bf16 v[120:123], v[16:19], v[184:187], v[120:123]\n\t" \
  "ds_read_b64_tr_b16 v[200:201], v229 offset:32256\n\t" \
  "ds_read_b64_tr_b16 v[202:203], v229 offset:34560\n\t" \
  "v_mfma_f32_16x16x32_bf16 v[124:127], v[20:23], v[184:187], v[124:127]\n\t" \
  "s_add_u32 m0, %[m2], 1024\n\t" \
  "s_nop 0\n\t" \
  "buffer_load_dwordx4 v233, %[rx], %[sx] offen lds\n\t" \
  "ds_read_b64_tr_b16 v[204:205], v229 offset:32296\n\t" \
  "s_waitcnt lgkmcnt(5)\n\t" \
  "s_cmp_eq_u32 %[ch], 0\n\t" \
  "s_cbranch_scc1 .Lp1f9_%=\n\t" \
  "v_mfma_f32_16x16x32_bf16 v[128:131], v[16:19], v[196:199], v[128:131]\n\t" \
  ".Lp1f9_%=:\n\t" \
  "ds_read_b64_tr_b16 v[206:207], v229 offset:34600\n\t" \
  "ds_read_b64_tr_b16 v[216:217], v231 offset:32256\n\t" \
  "s_cmp_eq_u32 %[ch], 0\n\t" \
  "s_cbranch_scc1 .Lp1f10_%=\n\t" \
  "v_mfma_f32_16x16x32_bf16 v[132:135], v[20:23], v[196:199], v[132:135]\n\t" \
  ".Lp1f10_%=:\n\t" \
  "ds_read_b64_tr_b16 v[218:219], v231 offset:34560\n\t" \
  "s_waitcnt vmcnt(3) lgkmcnt(6)\n\t" \
  "s_barrier\n\t" \
  "s_waitcnt lgkmcnt(4)\n\t" \
  "v_mfma_f32_16x16x32_bf16 v[112:115], v[24:27], v[200:203], v[112:115]\n\t" \
  "ds_read_b128 v[32:35], v220 offset:32768\n\t" \
  "v_mfma_f32_16x16x32_bf16 v[116:119], v[28:31], v[200:203], v[116:119]\n\t" \
  "s_add_u32 %[sx], %[sx], 128\n\t" \
  "s_cmp_eq_u32 %[sx], 0x480\n\t" \
  "s_cselect_b32 %[sx], 0, %[sx]\n\t" \
  "s_mul_i32 %[sw], %[sx], 0x6c0\n\t" \
  "s_add_u32 m0, %[m2], 16384\n\t" \
  "s_nop 0\n\t" \
  "buffer_load_dwordx4 v232, %[rx], %[sx] offen lds\n\t" \
  "ds_read_b128 v[36:39], v221 offset:32768\n\t" \
  "s_waitcnt lgkmcnt(4)\n\t" \
  "v_mfma_f32_16x16x32_bf16 v[120:123], v[24:27], v[204:207], v[120:123]\n\t" \
  "ds_read_b64_tr_b16 v[180:181], v229 offset:36864\n\t" \
  "ds_read_b64_tr_b16 v[182:183], v229 offset:39168\n\t" \
  "v_mfma_f32_16x16x32_bf16 v[124:127], v[28:31], v[204:207], v[124:127]\n\t" \
  "ds_read_b64_tr_b16 v[184:185], v229 offset:36904\n\t" \
  "s_waitcnt lgkmcnt(5)\n\t" \
  "s_cmp_eq_u32 %[ch], 0\n\t" \
  "s_cbranch_scc1 .Lp1f11_%=\n\t" \
  "v_mfma_f32_16x16x32_bf16 v[128:131], v[24:27], v[216:219], v[128:131]\n\t" \
  ".Lp1f11_%=:\n\t" \
  "s_add_u32 m0, %[m1], 122880\n\t" \
  "s_nop 0\n\t" \
  "buffer_load_dwordx4 v237, %[ra], %[sw] offen lds\n\t" \
  "s_cmp_lg_u32 %[m1], %[l0]\n\t" \
  "s_cbranch_scc1 .Lp1f12_%=\n\t" \
  "s_add_u32 m0, %[l0], 131072\n\t" \
  "s_nop 0\n\t" \
  "buffer_load_dwordx4 v238, %[ra], %[sw] offen lds\n\t" \
  ".Lp1f12_%=:\n\t" \
  "ds_read_b64_tr_b16 v[186:187], v229 offset:39208\n\t" \
  "ds_read_b64_tr_b16 v[196:197], v231 offset:36864\n\t" \
  "s_cmp_eq_u32 %[ch], 0\n\t" \
  "s_cbranch_scc1 .Lp1f13_%=\n\t" \
  "v_mfma_f32_16x16x32_bf16 v[132:135], v[28:31], v[216:219], v[132:135]\n\t" \
  ".Lp1f13_%=:\n\t" \
  "ds_read_b64_tr_b16 v[198:199], v231 offset:39168\n\t" \
  "s_waitcnt lgkmcnt(4)\n\t" \
  "v_mfma_f32_16x16x32_bf16 v[112:115], v[32:35], v[180:183], v[112:115]\n\t" \
  "ds_read_b128 v[40:43], v222 offset:32768\n\t" \
  "v_mfma_f32_16x16x32_bf16 v[116:119], v[36:39], v[180:183], v[116:119]\n\t" \
  "ds_read_b128 v[44:47], v223 offset:32768\n\t" \
  "s_waitcnt lgkmcnt(4)\n\t" \
  "v_mfma_f32_16x16x32_bf16 v[120:123], v[32:35], v[184:187], v[120:123]\n\t" \
  "ds_read_b64_tr_b16 v[200:201], v229 offset:41472\n\t" \
  "ds_read_b64_tr_b16 v[202:203], v229 offset:43776\n\t" \
  "v_mfma_f32_16x16x32_bf16 v[124:127], v[36:39], v[184:187], v[124:127]\n\t" \
  "s_add_u32 m0, %[m2], 17408\n\t" \
  "s_nop 0\n\t" \
  "buffer_load_dwordx4 v233, %[rx], %[sx] offen lds\n\t" \
  "ds_read_b64_tr_b16 v[204:205], v229 offset:41512\n\t" \
  "s_waitcnt lgkmcnt(5)\n\t" \
  "s_cmp_eq_u32 %[ch], 0\n\t" \
  "s_cbranch_scc1 .Lp1f14_%=\n\t" \
  "v_mfma_f32_16x16x32_bf16 v[128:131], v[32:35], v[196:199], v[128:131]\n\t" \
  ".Lp1f14_%=:\n\t" \
  "ds_read_b64_tr_b16 v[206:207], v229 offset:43816\n\t" \
  "ds_read_b64_tr_b16 v[216:217], v231 offset:41472\n\t" \
  "s_cmp_eq_u32 %[ch], 0\n\t" \
  "s_cbranch_scc1 .Lp1f15_%=\n\t" \
  "v_mfma_f32_16x16x32_bf16 v[132:135], v[36:39], v[196:199], v[132:135]\n\t" \
  ".Lp1f15_%=:\n\t" \
  "ds_read_b64_tr_b16 v[218:219], v231 offset:43776\n\t" \
  "s_waitcnt vmcnt(3) lgkmcnt(6)\n\t" \
  "s_barrier\n\t" \
  "s_waitcnt lgkmcnt(4)\n\t" \
  "v_mfma_f32_16x16x32_bf16 v[112:115], v[40:43], v[200:203], v[112:115]\n\t" \
  "ds_read_b128 v[48:51], v220 offset:0\n\t" \
  "v_mfma_f32_16x16x32_bf16 v[116:119], v[44:47], v[200:203], v[116:119]\n\t" \
  "s_add_u32 %[sx], %[sx], 128\n\t" \
  "s_cmp_eq_u32 %[sx], 0x480\n\t" \
  "s_cselect_b32 %[sx], 0, %[sx]\n\t" \
  "s_mul_i32 %[sw], %[sx], 0x6c0\n\t" \
  "s_add_u32 m0, %[m2], 32768\n\t" \
  "s_nop 0\n\t" \
  "buffer_load_dwordx4 v232, %[rx], %[sx] offen lds\n\t" \
  "ds_read_b128 v[52:55], v221 offset:0\n\t" \
  "s_waitcnt lgkmcnt(4)\n\t" \
  "v_mfma_f32_16x16x32_bf16 v[120:123], v[40:43], v[204:207], v[120:123]\n\t" \
  "ds_read_b64_tr_b16 v[180:181], v229 offset:46080\n\t" \
  "ds_read_b64_tr_b16 v[182:183], v229 offset:48384\n\t" \
  "v_mfma_f32_16x16x32_bf16 v[124:127], v[44:47], v[204:207], v[124:127]\n\t" \
  "ds_read_b64_tr_b16 v[184:185], v229 offset:46120\n\t" \
  "s_waitcnt lgkmcnt(5)\n\t" \
  "s_cmp_eq_u32 %[ch], 0\n\t" \
  "s_cbranch_scc1 .Lp1f16_%=\n\t" \
  "v_mfma_f32_16x16x32_bf16 v[128:131], v[40:43], v[216:219], v[128:131]\n\t" \
  ".Lp1f16_%=:\n\t" \
  "s_add_u32 m0, %[m1], 132096\n\t" \
  "s_nop 0\n\t" \
  "buffer_load_dwordx4 v237, %[ra], %[sw] offen lds\n\t" \
  "s_cmp_lg_u32 %[m1], %[l0]\n\t" \
  "s_cbranch_scc1 .Lp1f17_%=\n\t" \
  "s_add_u32 m0, %[l0], 140288\n\t" \
  "s_nop 0\n\t" \
  "buffer_load_dwordx4 v238, %[ra], %[sw] offen lds\n\t" \
  ".Lp1f17_%=:\n\t" \
  "ds_read_b64_tr_b16 v[186:187], v229 offset:48424\n\t" \
  "ds_read_b64_tr_b16 v[196:197], v231 offset:46080\n\t" \
  "s_cmp_eq_u32 %[ch], 0\n\t" \
  "s_cbranch_scc1 .Lp1f18_%=\n\t" \
  "v_mfma_f32_16x16x32_bf16 v[132:135], v[44:47], v[216:219], v[132:135]\n\t" \
  ".Lp1f18_%=:\n\t" \
  "ds_read_b64_tr_b16 v[198:199], v231 offset:48384\n\t" \
  "s_waitcnt lgkmcnt(4)\n\t" \
  "v_mfma_f32_16x16x32_bf16 v[112:115], v[48:51], v[180:183], v[112:115]\n\t" \
  "ds_read_b128 v[56:59], v222 offset:0\n\t" \
  "v_mfma_f32_16x16x32_bf16 v[116:119], v[52:55], v[180:183], v[116:119]\n\t" \
  "s_add_u32 m0, %[m2], 33792\n\t" \
  "s_nop 0\n\t" \
  "buffer_load_dwordx4 v233, %[rx], %[sx] offen lds\n\t" \
  "ds_read_b128 v[60:63], v223 offset:0\n\t" \
  "s_waitcnt lgkmcnt(4)\n\t" \
  "v_mfma_f32_16x16x32_bf16 v[120:123], v[48:51], v[184:187], v[120:123]\n\t" \
  "ds_read_b64_tr_b16 v[200:201], v229 offset:50688\n\t" \
  "ds_read_b64_tr_b16 v[202:203], v229 offset:52992\n\t" \
  "v_mfma_f32_16x16x32_bf16 v[124:127], v[52:55], v[184:187], v[124:127]\n\t" \
  "s_add_u32 m0, %[m2], 49152\n\t" \
  "s_nop 0\n\t" \
  "buffer_load_dwordx4 v234, %[rb], %[sb] offen lds\n\t" \
  "s_cmp_lg_u32 %[e2], %[l0]\n\t" \
  "s_cbranch_scc1 .Lp1f19_%=\n\t" \
  "s_add_u32 m0, %[m0q], 65536\n\t" \
  "s_nop 0\n\t" \
  "buffer_load_dwordx4 v236, %[rb], %[sb] offen lds\n\t" \
  ".Lp1f19_%=:\n\t" \
  "ds_read_b64_tr_b16 v[204:205], v229 offset:50728\n\t" \
  "s_waitcnt lgkmcnt(5)\n\t" \
  "s_cmp_eq_u32 %[ch], 0\n\t" \
  "s_cbranch_scc1 .Lp1f20_%=\n\t" \
  "v_mfma_f32_16x16x32_bf16 v[128:131], v[48:51], v[196:199], v[128:131]\n\t" \
  ".Lp1f20_%=:\n\t" \
  "ds_read_b64_tr_b16 v[206:207], v229 offset:53032\n\t" \
  "ds_read_b64_tr_b16 v[216:217], v231 offset:50688\n\t" \
  "s_cmp_eq_u32 %[ch], 0\n\t" \
  "s_cbranch_scc1 .Lp1f21_%=\n\t" \
  "v_mfma_f32_16x16x32_bf16 v[132:135], v[52:55], v[196:199], v[132:135]\n\t" \
  ".Lp1f21_%=:\n\t" \
  "s_add_u32 m0, %[m2], 50176\n\t" \
  "s_nop 0\n\t" \
  "buffer_load_dwordx4 v235, %[rb], %[sb] offen lds\n\t" \
  "ds_read_b64_tr_b16 v[218:219], v231 offset:52992\n\t" \
  "s_waitcnt vmcnt(5) lgkmcnt(6)\n\t" \
  "s_barrier\n\t" \
  "s_waitcnt lgkmcnt(4)\n\t" \
  "v_mfma_f32_16x16x32_bf16 v[112:115], v[56:59], v[200:203], v[112:115]\n\t" \
  "ds_read_b128 v[64:67], v220 offset:16384\n\t" \
  "v_mfma_f32_16x16x32_bf16 v[116:119], v[60:63], v[200:203], v[116:119]\n\t" \
  "s_add_u32 %[sx], %[sx], 128\n\t" \
  "s_cmp_eq_u32 %[sx], 0x480\n\t" \
  "s_cselect_b32 %[sx], 0, %[sx]\n\t" \
  "s_mul_i32 %[sw], %[sx], 0x6c0\n\t" \
  "s_add_u32 m0, %[m2], 0\n\t" \
  "s_nop 0\n\t" \
  "buffer_load_dwordx4 v232, %[rx], %[sx] offen lds\n\t" \
  "ds_read_b128 v[68:71], v221 offset:16384\n\t" \
  "s_waitcnt lgkmcnt(4)\n\t" \
  "v_mfma_f32_16x16x32_bf16 v[120:123], v[56:59], v[204:207], v[120:123]\n\t" \
  "ds_read_b64_tr_b16 v[180:181], v229 offset:18432\n\t" \
  "ds_read_b64_tr_b16 v[182:183], v229 offset:20736\n\t" \
  "v_mfma_f32_16x16x32_bf16 v[124:127], v[60:63], v[204:207], v[124:127]\n\t" \
  "ds_read_b64_tr_b16 v[184:185], v229 offset:18472\n\t" \
  "s_waitcnt lgkmcnt(5)\n\t" \
  "s_cmp_eq_u32 %[ch], 0\n\t" \
  "s_cbranch_scc1 .Lp1f22_%=\n\t" \
  "v_mfma_f32_16x16x32_bf16 v[128:131], v[56:59], v[216:219], v[128:131]\n\t" \
  ".Lp1f22_%=:\n\t" \
  "s_add_u32 m0, %[m1], 141312\n\t" \
  "s_nop 0\n\t" \
  "buffer_load_dwordx4 v237, %[ra], %[sw] offen lds\n\t" \
  "s_cmp_lg_u32 %[m1], %[l0]\n\t" \
  "s_cbranch_scc1 .Lp1f23_%=\n\t" \
  "s_add_u32 m0, %[l0], 149504\n\t" \
  "s_nop 0\n\t" \
  "buffer_load_dwordx4 v238, %[ra], %[sw] offen lds\n\t" \
  ".Lp1f23_%=:\n\t" \
  "ds_read_b64_tr_b16 v[186:187], v229 offset:20776\n\t" \
  "ds_read_b64_tr_b16 v[196:197], v231 offset:18432\n\t" \
  "s_cmp_eq_u32 %[ch], 0\n\t" \
  "s_cbranch_scc1 .Lp1f24_%=\n\t" \
  "v_mfma_f32_16x16x32_bf16 v[132:135], v[60:63], v[216:219], v[132:135]\n\t" \
  ".Lp1f24_%=:\n\t" \
  "ds_read_b64_tr_b16 v[198:199], v231 offset:20736\n\t" \
  "s_waitcnt lgkmcnt(4)\n\t" \
  "v_mfma_f32_16x16x32_bf16 v[112:115], v[64:67], v[180:183], v[112:115]\n\t" \
  "ds_read_b128 v[72:75], v222 offset:16384\n\t" \
  "v_mfma_f32_16x16x32_bf16 v[116:119], v[68:71], v[180:183], v[116:119]\n\t" \
  "s_add_u32 m0, %[m2], 1024\n\t" \
  "s_nop 0\n\t" \
  "buffer_load_dwordx4 v233, %[rx], %[sx] offen lds\n\t" \
  "ds_read_b128 v[76:79], v223 offset:16384\n\t" \
  "s_waitcnt lgkmcnt(4)\n\t" \
  "v_mfma_f32_16x16x32_bf16 v[120:123], v[64:67], v[184:187], v[120:123]\n\t" \
  "ds_read_b64_tr_b16 v[200:201], v229 offset:23040\n\t" \
  "ds_read_b64_tr_b16 v[202:203], v229 offset:25344\n\t" \
  "v_mfma_f32_16x16x32_bf16 v[124:127], v[68:71], v[184:187], v[124:127]\n\t" \
  "s_add_u32 %[sb], %[sb], 0x36000\n\t" \
  "s_cmp_eq_u32 %[sb], 0x1e6000\n\t" \
  "s_cselect_b32 %[sb], 0, %[sb]\n\t" \
  "s_add_u32 m0, %[m2], 67584\n\t" \
  "s_nop 0\n\t" \
  "buffer_load_dwordx4 v234, %[rb], %[sb] offen lds\n\t" \
  "s_cmp_lg_u32 %[e2], %[l0]\n\t" \
  "s_cbranch_scc1 .Lp1f25_%=\n\t" \
  "s_add_u32 m0, %[m0q], 83968\n\t" \
  "s_nop 0\n\t" \
  "buffer_load_dwordx4 v236, %[rb], %[sb] offen lds\n\t" \
  ".Lp1f25_%=:\n\t" \
  "ds_read_b64_tr_b16 v[204:205], v229 offset:23080\n\t" \
  "s_waitcnt lgkmcnt(5)\n\t" \
  "s_cmp_eq_u32 %[ch], 0\n\t" \
  "s_cbranch_scc1 .Lp1f26_%=\n\t" \
  "v_mfma_f32_16x16x32_bf16 v[128:131], v[64:67], v[196:199], v[128:131]\n\t" \
  ".Lp1f26_%=:\n\t" \
  "ds_read_b64_tr_b16 v[206:207], v229 offset:25384\n\t" \
  "ds_read_b64_tr_b16 v[216:217], v231 offset:23040\n\t" \
  "s_cmp_eq_u32 %[ch], 0\n\t" \
  "s_cbranch_scc1 .Lp1f27_%=\n\t" \
  "v_mfma_f32_16x16x32_bf16 v[132:135], v[68:71], v[196:199], v[132:135]\n\t" \
  ".Lp1f27_%=:\n\t" \
  "s_add_u32 m0, %[m2], 68608\n\t" \
  "s_nop 0\n\t" \
  "buffer_load_dwordx4 v235, %[rb], %[sb] offen lds\n\t" \
  "ds_read_b64_tr_b16 v[218:219], v231 offset:25344\n\t" \
  "s_waitcnt vmcnt(5) lgkmcnt(6)\n\t" \
  "s_barrier\n\t" \
  "s_waitcnt lgkmcnt(4)\n\t" \
  "v_mfma_f32_16x16x32_bf16 v[112:115], v[72:75], v[200:203], v[112:115]\n\t" \
  "ds_read_b128 v[80:83], v220 offset:32768\n\t" \
  "v_mfma_f32_16x16x32_bf16 v[116:119], v[76:79], v[200:203], v[116:119]\n\t" \
  "ds_read_b128 v[84:87], v221 offset:32768\n\t" \
  "s_waitcnt lgkmcnt(4)\n\t" \
  "v_mfma_f32_16x16x32_bf16 v[120:123], v[72:75], v[204:207], v[120:123]\n\t" \
  "ds_read_b64_tr_b16 v[180:181], v229 offset:27648\n\t" \
  "ds_read_b64_tr_b16 v[182:183], v229 offset:29952\n\t" \
  "v_mfma_f32_16x16x32_bf16 v[124:127], v[76:79], v[204:207], v[124:127]\n\t" \
  "s_add_u32 %[sx], %[sx], 128\n\t" \
  "s_cmp_eq_u32 %[sx], 0x480\n\t" \
  "s_cselect_b32 %[sx], 0, %[sx]\n\t" \
  "s_mul_i32 %[sw], %[sx], 0x6c0\n\t" \
  "s_add_u32 m0, %[m2], 16384\n\t" \
  "s_nop 0\n\t" \
  "buffer_load_dwordx4 v232, %[rx], %[sx] offen lds\n\t" \
  "ds_read_b64_tr_b16 v[184:185], v229 offset:27688\n\t" \
  "s_waitcnt lgkmcnt(5)\n\t" \
  "s_cmp_eq_u32 %[ch], 0\n\t" \
  "s_cbranch_scc1 .Lp1f28_%=\n\t" \
  "v_mfma_f32_16x16x32_bf16 v[128:131], v[72:75], v[216:219], v[128:131]\n\t" \
  ".Lp1f28_%=:\n\t" \
  "ds_read_b64_tr_b16 v[186:187], v229 offset:29992\n\t" \
  "ds_read_b64_tr_b16 v[196:197], v231 offset:27648\n\t" \
  "s_cmp_eq_u32 %[ch], 0\n\t" \
  "s_cbranch_scc1 .Lp1f29_%=\n\t" \
  "v_mfma_f32_16x16x32_bf16 v[132:135], v[76:79], v[216:219], v[132:135]\n\t" \
  ".Lp1f29_%=:\n\t" \
  "ds_read_b64_tr_b16 v[198:199], v231 offset:29952\n\t" \
  "s_waitcnt lgkmcnt(4)\n\t" \
  "v_mfma_f32_16x16x32_bf16 v[112:115], v[80:83], v[180:183], v[112:115]\n\t" \
  "ds_read_b128 v[88:91], v222 offset:32768\n\t" \
  "v_mfma_f32_16x16x32_bf16 v[116:119], v[84:87], v[180:183], v[116:119]\n\t" \
  "ds_read_b128 v[92:95], v223 offset:32768\n\t" \
  "s_waitcnt lgkmcnt(4)\n\t" \
  "v_mfma_f32_16x16x32_bf16 v[120:123], v[80:83], v[184:187], v[120:123]\n\t" \
  "ds_read_b64_tr_b16 v[200:201], v229 offset:32256\n\t" \
  "ds_read_b64_tr_b16 v[202:203], v229 offset:34560\n\t" \
  "v_mfma_f32_16x16x32_bf16 v[124:127], v[84:87], v[184:187], v[124:127]\n\t" \
  "s_add_u32 m0, %[m1], 150528\n\t" \
  "s_nop 0\n\t" \
  "buffer_load_dwordx4 v237, %[ra], %[sw] offen lds\n\t" \
  "s_cmp_lg_u32 %[m1], %[l0]\n\t" \
  "s_cbranch_scc1 .Lp1f30_%=\n\t" \
  "s_add_u32 m0, %[l0], 158720\n\t" \
  "s_nop 0\n\t" \
  "buffer_load_dwordx4 v238, %[ra], %[sw] offen lds\n\t" \
  ".Lp1f30_%=:\n\t" \
  "ds_read_b64_tr_b16 v[204:205], v229 offset:32296\n\t" \
  "s_waitcnt lgkmcnt(5)\n\t" \
  "s_cmp_eq_u32 %[ch], 0\n\t" \
  "s_cbranch_scc1 .Lp1f31_%=\n\t" \
  "v_mfma_f32_16x16x32_bf16 v[128:131], v[80:83], v[196:199], v[128:131]\n\t" \
  ".Lp1f31_%=:\n\t" \
  "ds_read_b64_tr_b16 v[206:207], v229 offset:34600\n\t" \
  "ds_read_b64_tr_b16 v[216:217], v231 offset:32256\n\t" \
  "s_cmp_eq_u32 %[ch], 0\n\t" \
  "s_cbranch_scc1 .Lp1f32_%=\n\t" \
  "v_mfma_f32_16x16x32_bf16 v[132:135], v[84:87], v[196:199], v[132:135]\n\t" \
  ".Lp1f32_%=:\n\t" \
  "ds_read_b64_tr_b16 v[218:219], v231 offset:34560\n\t" \
  "s_waitcnt lgkmcnt(4)\n\t" \
  "v_mfma_f32_16x16x32_bf16 v[112:115], v[88:91], v[200:203], v[112:115]\n\t" \
  "ds_read_b64_tr_b16 v[180:181], v224 offset:0\n\t" \
  "ds_read_b64_tr_b16 v[182:183], v224 offset:4608\n\t" \
  "v_mfma_f32_16x16x32_bf16 v[116:119], v[92:95], v[200:203], v[116:119]\n\t" \
  "ds_read_b64_tr_b16 v[184:185], v224 offset:40\n\t" \
  "ds_read_b64_tr_b16 v[186:187], v224 offset:4648\n\t" \
  "s_waitcnt lgkmcnt(6)\n\t" \
  "v_mfma_f32_16x16x32_bf16 v[120:123], v[88:91], v[204:207], v[120:123]\n\t" \
  "ds_read_b64_tr_b16 v[188:189], v224 offset:144\n\t" \
  "v_mfma_f32_16x16x32_bf16 v[124:127], v[92:95], v[204:207], v[124:127]\n\t" \
  "s_add_u32 m0, %[m2], 17408\n\t" \
  "s_nop 0\n\t" \
  "buffer_load_dwordx4 v233, %[rx], %[sx] offen lds\n\t" \
  "ds_read_b64_tr_b16 v[190:191], v224 offset:4752\n\t" \
  "ds_read_b64_tr_b16 v[192:193], v224 offset:184\n\t" \
  "s_waitcnt lgkmcnt(7)\n\t" \
  "s_cmp_eq_u32 %[ch], 0\n\t" \
  "s_cbranch_scc1 .Lp1f33_%=\n\t" \
  "v_mfma_f32_16x16x32_bf16 v[128:131], v[88:91], v[216:219], v[128:131]\n\t" \
  ".Lp1f33_%=:\n\t" \
  "ds_read_b64_tr_b16 v[194:195], v224 offset:4792\n\t" \
  "ds_read_b64_tr_b16 v[196:197], v226 offset:0\n\t" \
  "s_cmp_eq_u32 %[ch], 0\n\t" \
  "s_cbranch_scc1 .Lp1f34_%=\n\t" \
  "v_mfma_f32_16x16x32_bf16 v[132:135], v[92:95], v[216:219], v[132:135]\n\t" \
  ".Lp1f34_%=:\n\t" \
  "ds_read_b64_tr_b16 v[198:199], v226 offset:4608\n\t" \
  "s_waitcnt lgkmcnt(8)\n\t" \
  "v_mfma_f32_16x16x32_bf16 v[144:147], v[0:3], v[180:183], 0\n\t" \
  "ds_read_b64_tr_b16 v[200:201], v224 offset:9216\n\t" \
  "v_mfma_f32_16x16x32_bf16 v[148:151], v[4:7], v[180:183], 0\n\t" \
  "ds_read_b64_tr_b16 v[202:203], v224 offset:13824\n\t" \
  "s_waitcnt lgkmcnt(8)\n\t" \
  "v_mfma_f32_16x16x32_bf16 v[152:155], v[0:3], v[184:187], 0\n\t" \
  "s_add_u32 %[sb], %[sb], 0x36000\n\t" \
  "s_cmp_eq_u32 %[sb], 0x1e6000\n\t" \
  "s_cselect_b32 %[sb], 0, %[sb]\n\t" \
  "s_add_u32 m0, %[m2], 86016\n\t" \
  "s_nop 0\n\t" \
  "buffer_load_dwordx4 v234, %[rb], %[sb] offen lds\n\t" \
  "s_cmp_lg_u32 %[e2], %[l0]\n\t" \
  "s_cbranch_scc1 .Lp1f35_%=\n\t" \
  "s_add_u32 m0, %[m0q], 102400\n\t" \
  "s_nop 0\n\t" \
  "buffer_load_dwordx4 v236, %[rb], %[sb] offen lds\n\t" \
  ".Lp1f35_%=:\n\t" \
  "ds_read_b64_tr_b16 v[204:205], v224 offset:9256\n\t" \
  "ds_read_b64_tr_b16 v[206:207], v224 offset:13864\n\t" \
  "v_mfma_f32_16x16x32_bf16 v[156:159], v[4:7], v[184:187], 0\n\t" \
  "ds_read_b64_tr_b16 v[208:209], v224 offset:9360\n\t" \
  "s_waitcnt lgkmcnt(9)\n\t" \
  "v_mfma_f32_16x16x32_bf16 v[160:163], v[0:3], v[188:191], 0\n\t" \
  "ds_read_b64_tr_b16 v[210:211], v224 offset:13968\n\t" \
  "v_mfma_f32_16x16x32_bf16 v[164:167], v[4:7], v[188:191], 0\n\t" \
  "ds_read_b64_tr_b16 v[212:213], v224 offset:9400\n\t" \
  "s_waitcnt lgkmcnt(9)\n\t" \
  "v_mfma_f32_16x16x32_bf16 v[168:171], v[0:3], v[192:195], 0\n\t" \
  "s_add_u32 m0, %[m2], 87040\n\t" \
  "s_nop 0\n\t" \
  "buffer_load_dwordx4 v235, %[rb], %[sb] offen lds\n\t" \
  "ds_read_b64_tr_b16 v[214:215], v224 offset:14008\n\t" \
  "v_mfma_f32_16x16x32_bf16 v[172:175], v[4:7], v[192:195], 0\n\t" \
  "ds_read_b64_tr_b16 v[216:217], v226 offset:9216\n\t" \
  "s_waitcnt lgkmcnt(9)\n\t" \
  "v_mfma_f32_16x16x32_bf16 v[176:179], v[0:3], v[196:199], 0\n\t" \
  "ds_read_b64_tr_b16 v[218:219], v226 offset:13824\n\t" \
  "s_waitcnt vmcnt(5) lgkmcnt(10)\n\t" \
  "s_barrier\n\t" \
  "s_waitcnt lgkmcnt(8)\n\t" \
  "v_mfma_f32_16x16x32_bf16 v[144:147], v[8:11], v[200:203], v[144:147]\n\t" \
  "ds_read_b128 v[96:99], v220 offset:0\n\t" \
  "v_mfma_f32_16x16x32_bf16 v[148:151], v[12:15], v[200:203], v[148:151]\n\t" \
  "s_waitcnt lgkmcnt(7)\n\t" \
  "v_mfma_f32_16x16x32_bf16 v[152:155], v[8:11], v[204:207], v[152:155]\n\t" \
  "s_add_u32 %[sx], %[sx], 128\n\t" \
  "s_cmp_eq_u32 %[sx], 0x480\n\t" \
  "s_cselect_b32 %[sx], 0, %[sx]\n\t" \
  "s_mul_i32 %[sw], %[sx], 0x6c0\n\t" \
  "s_add_u32 m0, %[m2], 32768\n\t" \
  "s_nop 0\n\t" \
  "buffer_load_dwordx4 v232, %[rx], %[sx] offen lds\n\t" \
  "ds_read_b128 v[100:103], v221 offset:0\n\t" \
  "v_mfma_f32_16x16x32_bf16 v[156:159], v[12:15], v[204:207], v[156:159]\n\t" \
  "ds_read_b64_tr_b16 v[180:181], v229 offset:36864\n\t" \
  "s_waitcnt lgkmcnt(7)\n\t" \
  "v_mfma_f32_16x16x32_bf16 v[160:163], v[8:11], v[208:211], v[160:163]\n\t" \
  "ds_read_b64_tr_b16 v[182:183], v229 offset:39168\n\t" \
  "v_mfma_f32_16x16x32_bf16 v[164:167], v[12:15], v[208:211], v[164:167]\n\t" \
  "ds_read_b64_tr_b16 v[184:185], v229 offset:36904\n\t" \
  "s_waitcnt lgkmcnt(7)\n\t" \
  "v_mfma_f32_16x16x32_bf16 v[168:171], v[8:11], v[212:215], v[168:171]\n\t" \
  "s_add_u32 m0, %[m1], 122880\n\t" \
  "s_nop 0\n\t" \
  "buffer_load_dwordx4 v237, %[ra], %[sw] offen lds\n\t" \
  "s_cmp_lg_u32 %[m1], %[l0]\n\t" \
  "s_cbranch_scc1 .Lp1f36_%=\n\t" \
  "s_add_u32 m0, %[l0], 131072\n\t" \
  "s_nop 0\n\t" \
  "buffer_load_dwordx4 v238, %[ra], %[sw] offen lds\n\t" \
  ".Lp1f36_%=:\n\t" \
  "ds_read_b64_tr_b16 v[186:187], v229 offset:39208\n\t" \
  "v_mfma_f32_16x16x32_bf16 v[172:175], v[12:15], v[212:215], v[172:175]\n\t" \
  "ds_read_b64_tr_b16 v[196:197], v231 offset:36864\n\t" \
  "s_waitcnt lgkmcnt(7)\n\t" \
  "v_mfma_f32_16x16x32_bf16 v[176:179], v[8:11], v[216:219], v[176:179]\n\t" \
  "ds_read_b64_tr_b16 v[198:199], v231 offset:39168\n\t" \
  "s_waitcnt lgkmcnt(4)\n\t" \
  "v_mfma_f32_16x16x32_bf16 v[112:115], v[96:99], v[180:183], v[112:115]\n\t" \
  "ds_read_b128 v[104:107], v222 offset:0\n\t" \
  "v_mfma_f32_16x16x32_bf16 v[116:119], v[100:103], v[180:183], v[116:119]\n\t" \
  "ds_read_b128 v[108:111], v223 offset:0\n\t" \
  "s_waitcnt lgkmcnt(4)\n\t" \
  "v_mfma_f32_16x16x32_bf16 v[120:123], v[96:99], v[184:187], v[120:123]\n\t" \
  "ds_read_b64_tr_b16 v[200:201], v229 offset:41472\n\t" \
  "ds_read_b64_tr_b16 v[202:203], v229 offset:43776\n\t" \
  "v_mfma_f32_16x16x32_bf16 v[124:127], v[100:103], v[184:187], v[124:127]\n\t" \
  "s_add_u32 m0, %[m2], 33792\n\t" \
  "s_nop 0\n\t" \
  "buffer_load_dwordx4 v233, %[rx], %[sx] offen lds\n\t" \
  "ds_read_b64_tr_b16 v[204:205], v229 offset:41512\n\t" \
  "s_waitcnt lgkmcnt(5)\n\t" \
  "s_cmp_eq_u32 %[ch], 0\n\t" \
  "s_cbranch_scc1 .Lp1f37_%=\n\t" \
  "v_mfma_f32_16x16x32_bf16 v[128:131], v[96:99], v[196:199], v[128:131]\n\t" \
  ".Lp1f37_%=:\n\t" \
  "ds_read_b64_tr_b16 v[206:207], v229 offset:43816\n\t" \
  "ds_read_b64_tr_b16 v[216:217], v231 offset:41472\n\t" \
  "s_cmp_eq_u32 %[ch], 0\n\t" \
  "s_cbranch_scc1 .Lp1f38_%=\n\t" \
  "v_mfma_f32_16x16x32_bf16 v[132:135], v[100:103], v[196:199], v[132:135]\n\t" \
  ".Lp1f38_%=:\n\t" \
  "ds_read_b64_tr_b16 v[218:219], v231 offset:43776\n\t" \
  "s_waitcnt lgkmcnt(4)\n\t" \
  "v_mfma_f32_16x16x32_bf16 v[112:115], v[104:107], v[200:203], v[112:115]\n\t" \
  "ds_read_b64_tr_b16 v[180:181], v224 offset:18432\n\t" \
  "ds_read_b64_tr_b16 v[182:183], v224 offset:23040\n\t" \
  "v_mfma_f32_16x16x32_bf16 v[116:119], v[108:111], v[200:203], v[116:119]\n\t" \
  "ds_read_b64_tr_b16 v[184:185], v224 offset:18472\n\t" \
  "ds_read_b64_tr_b16 v[186:187], v224 offset:23080\n\t" \
  "s_waitcnt lgkmcnt(6)\n\t" \
  "v_mfma_f32_16x16x32_bf16 v[120:123], v[104:107], v[204:207], v[120:123]\n\t" \
  "ds_read_b64_tr_b16 v[188:189], v224 offset:18576\n\t" \
  "v_mfma_f32_16x16x32_bf16 v[124:127], v[108:111], v[204:207], v[124:127]\n\t" \
  "s_add_u32 %[sb], %[sb], 0x36000\n\t" \
  "s_cmp_eq_u32 %[sb], 0x1e6000\n\t" \
  "s_cselect_b32 %[sb], 0, %[sb]\n\t" \
  "s_add_u32 m0, %[m2], 104448\n\t" \
  "s_nop 0\n\t" \
  "buffer_load_dwordx4 v234, %[rb], %[sb] offen lds\n\t" \
  "s_cmp_lg_u32 %[e2], %[l0]\n\t" \
  "s_cbranch_scc1 .Lp1f39_%=\n\t" \
  "s_add_u32 m0, %[m0q], 120832\n\t" \
  "s_nop 0\n\t" \
  "buffer_load_dwordx4 v236, %[rb], %[sb] offen lds\n\t" \
  ".Lp1f39_%=:\n\t" \
  "ds_read_b64_tr_b16 v[190:191], v224 offset:23184\n\t" \
  "ds_read_b64_tr_b16 v[192:193], v224 offset:18616\n\t" \
  "s_waitcnt lgkmcnt(7)\n\t" \
  "s_cmp_eq_u32 %[ch], 0\n\t" \
  "s_cbranch_scc1 .Lp1f40_%=\n\t" \
  "v_mfma_f32_16x16x32_bf16 v[128:131], v[104:107], v[216:219], v[128:131]\n\t" \
  ".Lp1f40_%=:\n\t" \
  "ds_read_b64_tr_b16 v[194:195], v224 offset:23224\n\t" \
  "ds_read_b64_tr_b16 v[196:197], v226 offset:18432\n\t" \
  "s_cmp_eq_u32 %[ch], 0\n\t" \
  "s_cbranch_scc1 .Lp1f41_%=\n\t" \
  "v_mfma_f32_16x16x32_bf16 v[132:135], v[108:111], v[216:219], v[132:135]\n\t" \
  ".Lp1f41_%=:\n\t" \
  "ds_read_b64_tr_b16 v[198:199], v226 offset:23040\n\t" \
  "s_waitcnt lgkmcnt(8)\n\t" \
  "v_mfma_f32_16x16x32_bf16 v[144:147], v[16:19], v[180:183], v[144:147]\n\t" \
  "ds_read_b64_tr_b16 v[200:201], v224 offset:27648\n\t" \
  "v_mfma_f32_16x16x32_bf16 v[148:151], v[20:23], v[180:183], v[148:151]\n\t" \
  "ds_read_b64_tr_b16 v[202:203], v224 offset:32256\n\t" \
  "s_waitcnt lgkmcnt(8)\n\t" \
  "v_mfma_f32_16x16x32_bf16 v[152:155], v[16:19], v[184:187], v[152:155]\n\t" \
  "ds_read_b64_tr_b16 v[204:205], v224 offset:27688\n\t" \
  "ds_read_b64_tr_b16 v[206:207], v224 offset:32296\n\t" \
  "v_mfma_f32_16x16x32_bf16 v[156:159], v[20:23], v[184:187], v[156:159]\n\t" \
  "ds_read_b64_tr_b16 v[208:209], v224 offset:27792\n\t" \
  "s_waitcnt lgkmcnt(9)\n\t" \
  "v_mfma_f32_16x16x32_bf16 v[160:163], v[16:19], v[188:191], v[160:163]\n\t" \
  "s_add_u32 m0, %[m2], 105472\n\t" \
  "s_nop 0\n\t" \
  "buffer_load_dwordx4 v235, %[rb], %[sb] offen lds\n\t" \
  "ds_read_b64_tr_b16 v[210:211], v224 offset:32400\n\t" \
  "v_mfma_f32_16x16x32_bf16 v[164:167], v[20:23], v[188:191], v[164:167]\n\t" \
  "ds_read_b64_tr_b16 v[212:213], v224 offset:27832\n\t" \
  "s_waitcnt lgkmcnt(9)\n\t" \
  "v_mfma_f32_16x16x32_bf16 v[168:171], v[16:19], v[192:195], v[168:171]\n\t" \
  "ds_read_b64_tr_b16 v[214:215], v224 offset:32440\n\t" \
  "v_mfma_f32_16x16x32_bf16 v[172:175], v[20:23], v[192:195], v[172:175]\n\t" \
  "ds_read_b64_tr_b16 v[216:217], v226 offset:27648\n\t" \
  "s_waitcnt lgkmcnt(9)\n\t" \
  "v_mfma_f32_16x16x32_bf16 v[176:179], v[16:19], v[196:199], v[176:179]\n\t" \
  "ds_read_b64_tr_b16 v[218:219], v226 offset:32256\n\t" \
  "s_waitcnt vmcnt(5) lgkmcnt(10)\n\t" \
  "s_barrier\n\t" \
  "s_waitcnt lgkmcnt(8)\n\t" \
  "v_mfma_f32_16x16x32_bf16 v[144:147], v[24:27], v[200:203], v[144:147]\n\t" \
  "ds_read_b128 v[0:3], v220 offset:16384\n\t" \
  "v_mfma_f32_16x16x32_bf16 v[148:151], v[28:31], v[200:203], v[148:151]\n\t" \
  "s_waitcnt lgkmcnt(7)\n\t" \
  "v_mfma_f32_16x16x32_bf16 v[152:155], v[24:27], v[204:207], v[152:155]\n\t" \
  "ds_read_b128 v[4:7], v221 offset:16384\n\t" \
  "v_mfma_f32_16x16x32_bf16 v[156:159], v[28:31], v[204:207], v[156:159]\n\t" \
  "ds_read_b64_tr_b16 v[180:181], v229 offset:46080\n\t" \
  "s_waitcnt lgkmcnt(7)\n\t" \
  "v_mfma_f32_16x16x32_bf16 v[160:163], v[24:27], v[208:211], v[160:163]\n\t" \
  "s_add_u32 %[sb], %[sb], 0x36000\n\t" \
  "s_cmp_eq_u32 %[sb], 0x1e6000\n\t" \
  "s_cselect_b32 %[sb], 0, %[sb]\n\t" \
  "s_add_u32 m0, %[m2], 49152\n\t" \
  "s_nop 0\n\t" \
  "buffer_load_dwordx4 v234, %[rb], %[sb] offen lds\n\t" \
  "s_cmp_lg_u32 %[e2], %[l0]\n\t" \
  "s_cbranch_scc1 .Lp1f42_%=\n\t" \
  "s_add_u32 m0, %[m0q], 65536\n\t" \
  "s_nop 0\n\t" \
  "buffer_load_dwordx4 v236, %[rb], %[sb] offen lds\n\t" \
  ".Lp1f42_%=:\n\t" \
  "ds_read_b64_tr_b16 v[182:183], v229 offset:48384\n\t" \
  "v_mfma_f32_16x16x32_bf16 v[164:167], v[28:31], v[208:211], v[164:167]\n\t" \
  "ds_read_b64_tr_b16 v[184:185], v229 offset:46120\n\t" \
  "s_waitcnt lgkmcnt(7)\n\t" \
  "v_mfma_f32_16x16x32_bf16 v[168:171], v[24:27], v[212:215], v[168:171]\n\t" \
  "ds_read_b64_tr_b16 v[186:187], v229 offset:48424\n\t" \
  "v_mfma_f32_16x16x32_bf16 v[172:175], v[28:31], v[212:215], v[172:175]\n\t" \
  "ds_read_b64_tr_b16 v[196:197], v231 offset:46080\n\t" \
  "s_waitcnt lgkmcnt(7)\n\t" \
  "v_mfma_f32_16x16x32_bf16 v[176:179], v[24:27], v[216:219], v[176:179]\n\t" \
  "ds_read_b64_tr_b16 v[198:199], v231 offset:48384\n\t" \
  "s_waitcnt lgkmcnt(4)\n\t" \
  "v_mfma_f32_16x16x32_bf16 v[112:115], v[0:3], v[180:183], v[112:115]\n\t" \
  "ds_read_b128 v[8:11], v222 offset:16384\n\t" \
  "v_mfma_f32_16x16x32_bf16 v[116:119], v[4:7], v[180:183], v[116:119]\n\t" \
  "ds_read_b128 v[12:15], v223 offset:16384\n\t" \
  "s_waitcnt lgkmcnt(4)\n\t" \
  "v_mfma_f32_16x16x32_bf16 v[120:123], v[0:3], v[184:187], v[120:123]\n\t" \
  "ds_read_b64_tr_b16 v[200:201], v229 offset:50688\n\t" \
  "ds_read_b64_tr_b16 v[202:203], v229 offset:52992\n\t" \
  "v_mfma_f32_16x16x32_bf16 v[124:127], v[4:7], v[184:187], v[124:127]\n\t" \
  "ds_read_b64_tr_b16 v[204:205], v229 offset:50728\n\t" \
  "s_waitcnt lgkmcnt(5)\n\t" \
  "s_cmp_eq_u32 %[ch], 0\n\t" \
  "s_cbranch_scc1 .Lp1f43_%=\n\t" \
  "v_mfma_f32_16x16x32_bf16 v[128:131], v[0:3], v[196:199], v[128:131]\n\t" \
  ".Lp1f43_%=:\n\t" \
  "ds_read_b64_tr_b16 v[206:207], v229 offset:53032\n\t" \
  "ds_read_b64_tr_b16 v[216:217], v231 offset:50688\n\t" \
  "s_cmp_eq_u32 %[ch], 0\n\t" \
  "s_cbranch_scc1 .Lp1f44_%=\n\t" \
  "v_mfma_f32_16x16x32_bf16 v[132:135], v[4:7], v[196:199], v[132:135]\n\t" \
  ".Lp1f44_%=:\n\t" \
  "ds_read_b64_tr_b16 v[218:219], v231 offset:52992\n\t" \
  "s_waitcnt lgkmcnt(4)\n\t" \
  "v_mfma_f32_16x16x32_bf16 v[112:115], v[8:11], v[200:203], v[112:115]\n\t" \
  "ds_read_b64_tr_b16 v[180:181], v224 offset:36864\n\t" \
  "ds_read_b64_tr_b16 v[182:183], v224 offset:41472\n\t" \
  "v_mfma_f32_16x16x32_bf16 v[116:119], v[12:15], v[200:203], v[116:119]\n\t" \
  "ds_read_b64_tr_b16 v[184:185], v224 offset:36904\n\t" \
  "ds_read_b64_tr_b16 v[186:187], v224 offset:41512\n\t" \
  "s_waitcnt lgkmcnt(6)\n\t" \
  "v_mfma_f32_16x16x32_bf16 v[120:123], v[8:11], v[204:207], v[120:123]\n\t" \
  "ds_read_b64_tr_b16 v[188:189], v224 offset:37008\n\t" \
  "v_mfma_f32_16x16x32_bf16 v[124:127], v[12:15], v[204:207], v[124:127]\n\t" \
  "ds_read_b64_tr_b16 v[190:191], v224 offset:41616\n\t" \
  "ds_read_b64_tr_b16 v[192:193], v224 offset:37048\n\t" \
  "s_waitcnt lgkmcnt(7)\n\t" \
  "s_cmp_eq_u32 %[ch], 0\n\t" \
  "s_cbranch_scc1 .Lp1f45_%=\n\t" \
  "v_mfma_f32_16x16x32_bf16 v[128:131], v[8:11], v[216:219], v[128:131]\n\t" \
  ".Lp1f45_%=:\n\t" \
  "ds_read_b64_tr_b16 v[194:195], v224 offset:41656\n\t" \
  "ds_read_b64_tr_b16 v[196:197], v226 offset:36864\n\t" \
  "s_cmp_eq_u32 %[ch], 0\n\t" \
  "s_cbranch_scc1 .Lp1f46_%=\n\t" \
  "v_mfma_f32_16x16x32_bf16 v[132:135], v[12:15], v[216:219], v[132:135]\n\t" \
  ".Lp1f46_%=:\n\t" \
  "ds_read_b64_tr_b16 v[198:199], v226 offset:41472\n\t" \
  "s_waitcnt lgkmcnt(8)\n\t" \
  "v_mfma_f32_16x16x32_bf16 v[144:147], v[32:35], v[180:183], v[144:147]\n\t" \
  "ds_read_b64_tr_b16 v[200:201], v224 offset:46080\n\t" \
  "v_mfma_f32_16x16x32_bf16 v[148:151], v[36:39], v[180:183], v[148:151]\n\t" \
  "ds_read_b64_tr_b16 v[202:203], v224 offset:50688\n\t" \
  "s_waitcnt lgkmcnt(8)\n\t" \
  "v_mfma_f32_16x16x32_bf16 v[152:155], v[32:35], v[184:187], v[152:155]\n\t" \
  "ds_read_b64_tr_b16 v[204:205], v224 offset:46120\n\t" \
  "ds_read_b64_tr_b16 v[206:207], v224 offset:50728\n\t" \
  "v_mfma_f32_16x16x32_bf16 v[156:159], v[36:39], v[184:187], v[156:159]\n\t" \
  "ds_read_b64_tr_b16 v[208:209], v224 offset:46224\n\t" \
  "s_waitcnt lgkmcnt(9)\n\t" \
  "v_mfma_f32_16x16x32_bf16 v[160:163], v[32:35], v[188:191], v[160:163]\n\t" \
  "s_add_u32 m0, %[m2], 50176\n\t" \
  "s_nop 0\n\t" \
  "buffer_load_dwordx4 v235, %[rb], %[sb] offen lds\n\t" \
  "ds_read_b64_tr_b16 v[210:211], v224 offset:50832\n\t" \
  "v_mfma_f32_16x16x32_bf16 v[164:167], v[36:39], v[188:191], v[164:167]\n\t" \
  "ds_read_b64_tr_b16 v[212:213], v224 offset:46264\n\t" \
  "s_waitcnt lgkmcnt(9)\n\t" \
  "v_mfma_f32_16x16x32_bf16 v[168:171], v[32:35], v[192:195], v[168:171]\n\t" \
  "ds_read_b64_tr_b16 v[214:215], v224 offset:50872\n\t" \
  "v_mfma_f32_16x16x32_bf16 v[172:175], v[36:39], v[192:195], v[172:175]\n\t" \
  "ds_read_b64_tr_b16 v[216:217], v226 offset:46080\n\t" \
  "s_waitcnt lgkmcnt(9)\n\t" \
  "v_mfma_f32_16x16x32_bf16 v[176:179], v[32:35], v[196:199], v[176:179]\n\t" \
  "ds_read_b64_tr_b16 v[218:219], v226 offset:50688\n\t" \
  "s_waitcnt vmcnt(2) lgkmcnt(10)\n\t" \
  "s_barrier\n\t" \
  "s_waitcnt lgkmcnt(8)\n\t" \
  "v_mfma_f32_16x16x32_bf16 v[144:147], v[40:43], v[200:203], v[144:147]\n\t" \
  "ds_read_b128 v[16:19], v220 offset:32768\n\t" \
  "v_mfma_f32_16x16x32_bf16 v[148:151], v[44:47], v[200:203], v[148:151]\n\t" \
  "s_waitcnt lgkmcnt(7)\n\t" \
  "v_mfma_f32_16x16x32_bf16 v[152:155], v[40:43], v[204:207], v[152:155]\n\t" \
  "ds_read_b128 v[20:23], v221 offset:32768\n\t" \
  "v_mfma_f32_16x16x32_bf16 v[156:159], v[44:47], v[204:207], v[156:159]\n\t" \
  "ds_read_b64_tr_b16 v[180:181], v229 offset:18432\n\t" \
  "s_waitcnt lgkmcnt(7)\n\t" \
  "v_mfma_f32_16x16x32_bf16 v[160:163], v[40:43], v[208:211], v[160:163]\n\t" \
  "s_add_u32 %[sb], %[sb], 0x36000\n\t" \
  "s_cmp_eq_u32 %[sb], 0x1e6000\n\t" \
  "s_cselect_b32 %[sb], 0, %[sb]\n\t" \
  "s_add_u32 m0, %[m2], 67584\n\t" \
  "s_nop 0\n\t" \
  "buffer_load_dwordx4 v234, %[rb], %[sb] offen lds\n\t" \
  "s_cmp_lg_u32 %[e2], %[l0]\n\t" \
  "s_cbranch_scc1 .Lp1f47_%=\n\t" \
  "s_add_u32 m0, %[m0q], 83968\n\t" \
  "s_nop 0\n\t" \
  "buffer_load_dwordx4 v236, %[rb], %[sb] offen lds\n\t" \
  ".Lp1f47_%=:\n\t" \
  "ds_read_b64_tr_b16 v[182:183], v229 offset:20736\n\t" \
  "v_mfma_f32_16x16x32_bf16 v[164:167], v[44:47], v[208:211], v[164:167]\n\t" \
  "ds_read_b64_tr_b16 v[184:185], v229 offset:18472\n\t" \
  "s_waitcnt lgkmcnt(7)\n\t" \
  "v_mfma_f32_16x16x32_bf16 v[168:171], v[40:43], v[212:215], v[168:171]\n\t" \
  "ds_read_b64_tr_b16 v[186:187], v229 offset:20776\n\t" \
  "v_mfma_f32_16x16x32_bf16 v[172:175], v[44:47], v[212:215], v[172:175]\n\t" \
  "ds_read_b64_tr_b16 v[196:197], v231 offset:18432\n\t" \
  "s_waitcnt lgkmcnt(7)\n\t" \
  "v_mfma_f32_16x16x32_bf16 v[176:179], v[40:43], v[216:219], v[176:179]\n\t" \
  "ds_read_b64_tr_b16 v[198:199], v231 offset:20736\n\t" \
  "s_waitcnt lgkmcnt(4)\n\t" \
  "v_mfma_f32_16x16x32_bf16 v[112:115], v[16:19], v[180:183], v[112:115]\n\t" \
  "ds_read_b128 v[24:27], v222 offset:32768\n\t" \
  "v_mfma_f32_16x16x32_bf16 v[116:119], v[20:23], v[180:183], v[116:119]\n\t" \
  "ds_read_b128 v[28:31], v223 offset:32768\n\t" \
  "s_waitcnt lgkmcnt(4)\n\t" \
  "v_mfma_f32_16x16x32_bf16 v[120:123], v[16:19], v[184:187], v[120:123]\n\t" \
  "ds_read_b64_tr_b16 v[200:201], v229 offset:23040\n\t" \
  "ds_read_b64_tr_b16 v[202:203], v229 offset:25344\n\t" \
  "v_mfma_f32_16x16x32_bf16 v[124:127], v[20:23], v[184:187], v[124:127]\n\t" \
  "ds_read_b64_tr_b16 v[204:205], v229 offset:23080\n\t" \
  "s_waitcnt lgkmcnt(5)\n\t" \
  "s_cmp_eq_u32 %[ch], 0\n\t" \
  "s_cbranch_scc1 .Lp1f48_%=\n\t" \
  "v_mfma_f32_16x16x32_bf16 v[128:131], v[16:19], v[196:199], v[128:131]\n\t" \
  ".Lp1f48_%=:\n\t" \
  "ds_read_b64_tr_b16 v[206:207], v229 offset:25384\n\t" \
  "ds_read_b64_tr_b16 v[216:217], v231 offset:23040\n\t" \
  "s_cmp_eq_u32 %[ch], 0\n\t" \
  "s_cbranch_scc1 .Lp1f49_%=\n\t" \
  "v_mfma_f32_16x16x32_bf16 v[132:135], v[20:23], v[196:199], v[132:135]\n\t" \
  ".Lp1f49_%=:\n\t" \
  "ds_read_b64_tr_b16 v[218:219], v231 offset:25344\n\t" \
  "s_waitcnt lgkmcnt(4)\n\t" \
  "v_mfma_f32_16x16x32_bf16 v[112:115], v[24:27], v[200:203], v[112:115]\n\t" \
  "ds_read_b64_tr_b16 v[180:181], v225 offset:0\n\t" \
  "ds_read_b64_tr_b16 v[182:183], v225 offset:4608\n\t" \
  "v_mfma_f32_16x16x32_bf16 v[116:119], v[28:31], v[200:203], v[116:119]\n\t" \
  "ds_read_b64_tr_b16 v[184:185], v225 offset:40\n\t" \
  "ds_read_b64_tr_b16 v[186:187], v225 offset:4648\n\t" \
  "s_waitcnt lgkmcnt(6)\n\t" \
  "v_mfma_f32_16x16x32_bf16 v[120:123], v[24:27], v[204:207], v[120:123]\n\t" \
  "ds_read_b64_tr_b16 v[188:189], v225 offset:144\n\t" \
  "v_mfma_f32_16x16x32_bf16 v[124:127], v[28:31], v[204:207], v[124:127]\n\t" \
  "ds_read_b64_tr_b16 v[190:191], v225 offset:4752\n\t" \
  "ds_read_b64_tr_b16 v[192:193], v225 offset:184\n\t" \
  "s_waitcnt lgkmcnt(7)\n\t" \
  "s_cmp_eq_u32 %[ch], 0\n\t" \
  "s_cbranch_scc1 .Lp1f50_%=\n\t" \
  "v_mfma_f32_16x16x32_bf16 v[128:131], v[24:27], v[216:219], v[128:131]\n\t" \
  ".Lp1f50_%=:\n\t" \
  "ds_read_b64_tr_b16 v[194:195], v225 offset:4792\n\t" \
  "ds_read_b64_tr_b16 v[196:197], v227 offset:0\n\t" \
  "s_cmp_eq_u32 %[ch], 0\n\t" \
  "s_cbranch_scc1 .Lp1f51_%=\n\t" \
  "v_mfma_f32_16x16x32_bf16 v[132:135], v[28:31], v[216:219], v[132:135]\n\t" \
  ".Lp1f51_%=:\n\t" \
  "ds_read_b64_tr_b16 v[198:199], v227 offset:4608\n\t" \
  "s_waitcnt lgkmcnt(0)\n\t" \
  "s_nop 7\n\t"

#define P1B_F \
  "v_mfma_f32_16x16x32_bf16 v[144:147], v[48:51], v[180:183], v[144:147]\n\t" \
  "ds_read_b64_tr_b16 v[200:201], v225 offset:9216\n\t" \
  "v_mfma_f32_16x16x32_bf16 v[148:151], v[52:55], v[180:183], v[148:151]\n\t" \
  "ds_read_b64_tr_b16 v[202:203], v225 offset:13824\n\t" \
  "v_mfma_f32_16x16x32_bf16 v[152:155], v[48:51], v[184:187], v[152:155]\n\t" \
  "ds_read_b64_tr_b16 v[204:205], v225 offset:9256\n\t" \
  "ds_read_b64_tr_b16 v[206:207], v225 offset:13864\n\t" \
  "v_mfma_f32_16x16x32_bf16 v[156:159], v[52:55], v[184:187], v[156:159]\n\t" \
  "ds_read_b64_tr_b16 v[208:209], v225 offset:9360\n\t" \
  "v_mfma_f32_16x16x32_bf16 v[160:163], v[48:51], v[188:191], v[160:163]\n\t" \
  "s_add_u32 m0, %[m2], 68608\n\t" \
  "s_nop 0\n\t" \
  "buffer_load_dwordx4 v235, %[rb], %[sb] offen lds\n\t" \
  "ds_read_b64_tr_b16 v[210:211], v225 offset:13968\n\t" \
  "v_mfma_f32_16x16x32_bf16 v[164:167], v[52:55], v[188:191], v[164:167]\n\t" \
  "ds_read_b64_tr_b16 v[212:213], v225 offset:9400\n\t" \
  "v_mfma_f32_16x16x32_bf16 v[168:171], v[48:51], v[192:195], v[168:171]\n\t" \
  "ds_read_b64_tr_b16 v[214:215], v225 offset:14008\n\t" \
  "v_mfma_f32_16x16x32_bf16 v[172:175], v[52:55], v[192:195], v[172:175]\n\t" \
  "ds_read_b64_tr_b16 v[216:217], v227 offset:9216\n\t" \
  "v_mfma_f32_16x16x32_bf16 v[176:179], v[48:51], v[196:199], v[176:179]\n\t" \
  "ds_read_b64_tr_b16 v[218:219], v227 offset:13824\n\t" \
  "s_waitcnt vmcnt(4) lgkmcnt(10)\n\t" \
  "s_barrier\n\t" \
  "s_waitcnt lgkmcnt(8)\n\t" \
  "v_mfma_f32_16x16x32_bf16 v[144:147], v[56:59], v[200:203], v[144:147]\n\t" \
  "ds_read_b64_tr_b16 v[180:181], v224 offset:0\n\t" \
  "v_mfma_f32_16x16x32_bf16 v[148:151], v[60:63], v[200:203], v[148:151]\n\t" \
  "ds_read_b64_tr_b16 v[182:183], v224 offset:4608\n\t" \
  "s_waitcnt lgkmcnt(8)\n\t" \
  "v_mfma_f32_16x16x32_bf16 v[152:155], v[56:59], v[204:207], v[152:155]\n\t" \
  "s_add_u32 %[sb], %[sb], 0x36000\n\t" \
  "s_cmp_eq_u32 %[sb], 0x1e6000\n\t" \
  "s_cselect_b32 %[sb], 0, %[sb]\n\t" \
  "s_add_u32 m0, %[m2], 86016\n\t" \
  "s_nop 0\n\t" \
  "buffer_load_dwordx4 v234, %[rb], %[sb] offen lds\n\t" \
  "s_cmp_lg_u32 %[e2], %[l0]\n\t" \
  "s_cbranch_scc1 .Lp1f52_%=\n\t" \
  "s_add_u32 m0, %[m0q], 102400\n\t" \
  "s_nop 0\n\t" \
  "buffer_load_dwordx4 v236, %[rb], %[sb] offen lds\n\t" \
  ".Lp1f52_%=:\n\t" \
  "ds_read_b64_tr_b16 v[184:185], v224 offset:40\n\t" \
  "ds_read_b64_tr_b16 v[186:187], v224 offset:4648\n\t" \
  "v_mfma_f32_16x16x32_bf16 v[156:159], v[60:63], v[204:207], v[156:159]\n\t" \
  "ds_read_b64_tr_b16 v[188:189], v224 offset:144\n\t" \
  "s_waitcnt lgkmcnt(9)\n\t" \
  "v_mfma_f32_16x16x32_bf16 v[160:163], v[56:59], v[208:211], v[160:163]\n\t" \
  "ds_read_b64_tr_b16 v[190:191], v224 offset:4752\n\t" \
  "v_mfma_f32_16x16x32_bf16 v[164:167], v[60:63], v[208:211], v[164:167]\n\t" \
  "ds_read_b64_tr_b16 v[192:193], v224 offset:184\n\t" \
  "s_waitcnt lgkmcnt(9)\n\t" \
  "v_mfma_f32_16x16x32_bf16 v[168:171], v[56:59], v[212:215], v[168:171]\n\t" \
  "s_add_u32 m0, %[m2], 87040\n\t" \
  "s_nop 0\n\t" \
  "buffer_load_dwordx4 v235, %[rb], %[sb] offen lds\n\t" \
  "ds_read_b64_tr_b16 v[194:195], v224 offset:4792\n\t" \
  "v_mfma_f32_16x16x32_bf16 v[172:175], v[60:63], v[212:215], v[172:175]\n\t" \
  "ds_read_b64_tr_b16 v[196:197], v226 offset:0\n\t" \
  "s_waitcnt lgkmcnt(9)\n\t" \
  "v_mfma_f32_16x16x32_bf16 v[176:179], v[56:59], v[216:219], v[176:179]\n\t" \
  "ds_read_b64_tr_b16 v[198:199], v226 offset:4608\n\t" \
  "s_waitcnt lgkmcnt(8)\n\t" \
  "v_mfma_f32_16x16x32_bf16 v[144:147], v[64:67], v[180:183], v[144:147]\n\t" \
  "ds_read_b64_tr_b16 v[200:201], v224 offset:9216\n\t" \
  "v_mfma_f32_16x16x32_bf16 v[148:151], v[68:71], v[180:183], v[148:151]\n\t" \
  "ds_read_b64_tr_b16 v[202:203], v224 offset:13824\n\t" \
  "s_waitcnt lgkmcnt(8)\n\t" \
  "v_mfma_f32_16x16x32_bf16 v[152:155], v[64:67], v[184:187], v[152:155]\n\t" \
  "s_add_u32 %[sb], %[sb], 0x36000\n\t" \
  "s_cmp_eq_u32 %[sb], 0x1e6000\n\t" \
  "s_cselect_b32 %[sb], 0, %[sb]\n\t" \
  "s_add_u32 m0, %[m2], 122880\n\t" \
  "s_nop 0\n\t" \
  "buffer_load_dwordx4 v234, %[rb], %[sb] offen lds\n\t" \
  "s_cmp_lg_u32 %[e2], %[l0]\n\t" \
  "s_cbranch_scc1 .Lp1f53_%=\n\t" \
  "s_add_u32 m0, %[m0q], 139264\n\t" \
  "s_nop 0\n\t" \
  "buffer_load_dwordx4 v236, %[rb], %[sb] offen lds\n\t" \
  ".Lp1f53_%=:\n\t" \
  "ds_read_b64_tr_b16 v[204:205], v224 offset:9256\n\t" \
  "ds_read_b64_tr_b16 v[206:207], v224 offset:13864\n\t" \
  "v_mfma_f32_16x16x32_bf16 v[156:159], v[68:71], v[184:187], v[156:159]\n\t" \
  "ds_read_b64_tr_b16 v[208:209], v224 offset:9360\n\t" \
  "s_waitcnt lgkmcnt(9)\n\t" \
  "v_mfma_f32_16x16x32_bf16 v[160:163], v[64:67], v[188:191], v[160:163]\n\t" \
  "ds_read_b64_tr_b16 v[210:211], v224 offset:13968\n\t" \
  "v_mfma_f32_16x16x32_bf16 v[164:167], v[68:71], v[188:191], v[164:167]\n\t" \
  "ds_read_b64_tr_b16 v[212:213], v224 offset:9400\n\t" \
  "s_waitcnt lgkmcnt(9)\n\t" \
  "v_mfma_f32_16x16x32_bf16 v[168:171], v[64:67], v[192:195], v[168:171]\n\t" \
  "s_add_u32 m0, %[m2], 123904\n\t" \
  "s_nop 0\n\t" \
  "buffer_load_dwordx4 v235, %[rb], %[sb] offen lds\n\t" \
  "ds_read_b64_tr_b16 v[214:215], v224 offset:14008\n\t" \
  "v_mfma_f32_16x16x32_bf16 v[172:175], v[68:71], v[192:195], v[172:175]\n\t" \
  "ds_read_b64_tr_b16 v[216:217], v226 offset:9216\n\t" \
  "s_waitcnt lgkmcnt(9)\n\t" \
  "v_mfma_f32_16x16x32_bf16 v[176:179], v[64:67], v[196:199], v[176:179]\n\t" \
  "ds_read_b64_tr_b16 v[218:219], v226 offset:13824\n\t" \
  "s_waitcnt vmcnt(4) lgkmcnt(10)\n\t" \
  "s_barrier\n\t" \
  "s_waitcnt lgkmcnt(8)\n\t" \
  "v_mfma_f32_16x16x32_bf16 v[144:147], v[72:75], v[200:203], v[144:147]\n\t" \
  "ds_read_b64_tr_b16 v[180:181], v224 offset:18432\n\t" \
  "v_mfma_f32_16x16x32_bf16 v[148:151], v[76:79], v[200:203], v[148:151]\n\t" \
  "ds_read_b64_tr_b16 v[182:183], v224 offset:23040\n\t" \
  "s_waitcnt lgkmcnt(8)\n\t" \
  "v_mfma_f32_16x16x32_bf16 v[152:155], v[72:75], v[204:207], v[152:155]\n\t" \
  "s_add_u32 %[sb], %[sb], 0x36000\n\t" \
  "s_cmp_eq_u32 %[sb], 0x1e6000\n\t" \
  "s_cselect_b32 %[sb], 0, %[sb]\n\t" \
  "s_add_u32 m0, %[m2], 104448\n\t" \
  "s_nop 0\n\t" \
  "buffer_load_dwordx4 v234, %[rb], %[sb] offen lds\n\t" \
  "s_cmp_lg_u32 %[e2], %[l0]\n\t" \
  "s_cbranch_scc1 .Lp1f54_%=\n\t" \
  "s_add_u32 m0, %[m0q], 120832\n\t" \
  "s_nop 0\n\t" \
  "buffer_load_dwordx4 v236, %[rb], %[sb] offen lds\n\t" \
  ".Lp1f54_%=:\n\t" \
  "ds_read_b64_tr_b16 v[184:185], v224 offset:18472\n\t" \
  "ds_read_b64_tr_b16 v[186:187], v224 offset:23080\n\t" \
  "v_mfma_f32_16x16x32_bf16 v[156:159], v[76:79], v[204:207], v[156:159]\n\t" \
  "ds_read_b64_tr_b16 v[188:189], v224 offset:18576\n\t" \
  "s_waitcnt lgkmcnt(9)\n\t" \
  "v_mfma_f32_16x16x32_bf16 v[160:163], v[72:75], v[208:211], v[160:163]\n\t" \
  "ds_read_b64_tr_b16 v[190:191], v224 offset:23184\n\t" \
  "v_mfma_f32_16x16x32_bf16 v[164:167], v[76:79], v[208:211], v[164:167]\n\t" \
  "ds_read_b64_tr_b16 v[192:193], v224 offset:18616\n\t" \
  "s_waitcnt lgkmcnt(9)\n\t" \
  "v_mfma_f32_16x16x32_bf16 v[168:171], v[72:75], v[212:215], v[168:171]\n\t" \
  "s_add_u32 m0, %[m2], 105472\n\t" \
  "s_nop 0\n\t" \
  "buffer_load_dwordx4 v235, %[rb], %[sb] offen lds\n\t" \
  "ds_read_b64_tr_b16 v[194:195], v224 offset:23224\n\t" \
  "v_mfma_f32_16x16x32_bf16 v[172:175], v[76:79], v[212:215], v[172:175]\n\t" \
  "ds_read_b64_tr_b16 v[196:197], v226 offset:18432\n\t" \
  "s_waitcnt lgkmcnt(9)\n\t" \
  "v_mfma_f32_16x16x32_bf16 v[176:179], v[72:75], v[216:219], v[176:179]\n\t" \
  "ds_read_b64_tr_b16 v[198:199], v226 offset:23040\n\t" \
  "s_waitcnt lgkmcnt(8)\n\t" \
  "v_mfma_f32_16x16x32_bf16 v[144:147], v[80:83], v[180:183], v[144:147]\n\t" \
  "ds_read_b64_tr_b16 v[200:201], v224 offset:27648\n\t" \
  "v_mfma_f32_16x16x32_bf16 v[148:151], v[84:87], v[180:183], v[148:151]\n\t" \
  "ds_read_b64_tr_b16 v[202:203], v224 offset:32256\n\t" \
  "s_waitcnt lgkmcnt(8)\n\t" \
  "v_mfma_f32_16x16x32_bf16 v[152:155], v[80:83], v[184:187], v[152:155]\n\t" \
  "ds_read_b64_tr_b16 v[204:205], v224 offset:27688\n\t" \
  "ds_read_b64_tr_b16 v[206:207], v224 offset:32296\n\t" \
  "v_mfma_f32_16x16x32_bf16 v[156:159], v[84:87], v[184:187], v[156:159]\n\t" \
  "ds_read_b64_tr_b16 v[208:209], v224 offset:27792\n\t" \
  "s_waitcnt lgkmcnt(9)\n\t" \
  "v_mfma_f32_16x16x32_bf16 v[160:163], v[80:83], v[188:191], v[160:163]\n\t" \
  "ds_read_b64_tr_b16 v[210:211], v224 offset:32400\n\t" \
  "v_mfma_f32_16x16x32_bf16 v[164:167], v[84:87], v[188:191], v[164:167]\n\t" \
  "ds_read_b64_tr_b16 v[212:213], v224 offset:27832\n\t" \
  "s_waitcnt lgkmcnt(9)\n\t" \
  "v_mfma_f32_16x16x32_bf16 v[168:171], v[80:83], v[192:195], v[168:171]\n\t" \
  "ds_read_b64_tr_b16 v[214:215], v224 offset:32440\n\t" \
  "v_mfma_f32_16x16x32_bf16 v[172:175], v[84:87], v[192:195], v[172:175]\n\t" \
  "ds_read_b64_tr_b16 v[216:217], v226 offset:27648\n\t" \
  "s_waitcnt lgkmcnt(9)\n\t" \
  "v_mfma_f32_16x16x32_bf16 v[176:179], v[80:83], v[196:199], v[176:179]\n\t" \
  "ds_read_b64_tr_b16 v[218:219], v226 offset:32256\n\t" \
  "s_waitcnt vmcnt(0) lgkmcnt(0)\n\t" \
  "s_barrier\n\t" \
  "s_cmp_lg_u32 %[done], 2\n\t" \
  "s_cbranch_scc1 .Lp1f55_%=\n\t" \
  "global_store_dwordx2 %[vz], %[tag], %[fl] sc1\n\t" \
  ".Lp1f55_%=:\n\t" \
  "v_mfma_f32_16x16x32_bf16 v[144:147], v[88:91], v[200:203], v[144:147]\n\t" \
  "ds_read_b64_tr_b16 v[180:181], v224 offset:36864\n\t" \
  "v_mfma_f32_16x16x32_bf16 v[148:151], v[92:95], v[200:203], v[148:151]\n\t" \
  "ds_read_b64_tr_b16 v[182:183], v224 offset:41472\n\t" \
  "v_mfma_f32_16x16x32_bf16 v[152:155], v[88:91], v[204:207], v[152:155]\n\t" \
  "ds_read_b64_tr_b16 v[184:185], v224 offset:36904\n\t" \
  "ds_read_b64_tr_b16 v[186:187], v224 offset:41512\n\t" \
  "v_mfma_f32_16x16x32_bf16 v[156:159], v[92:95], v[204:207], v[156:159]\n\t" \
  "ds_read_b64_tr_b16 v[188:189], v224 offset:37008\n\t" \
  "v_mfma_f32_16x16x32_bf16 v[160:163], v[88:91], v[208:211], v[160:163]\n\t" \
  "ds_read_b64_tr_b16 v[190:191], v224 offset:41616\n\t" \
  "v_mfma_f32_16x16x32_bf16 v[164:167], v[92:95], v[208:211], v[164:167]\n\t" \
  "ds_read_b64_tr_b16 v[192:193], v224 offset:37048\n\t" \
  "v_mfma_f32_16x16x32_bf16 v[168:171], v[88:91], v[212:215], v[168:171]\n\t" \
  "ds_read_b64_tr_b16 v[194:195], v224 offset:41656\n\t" \
  "v_mfma_f32_16x16x32_bf16 v[172:175], v[92:95], v[212:215], v[172:175]\n\t" \
  "ds_read_b64_tr_b16 v[196:197], v226 offset:36864\n\t" \
  "v_mfma_f32_16x16x32_bf16 v[176:179], v[88:91], v[216:219], v[176:179]\n\t" \
  "ds_read_b64_tr_b16 v[198:199], v226 offset:41472\n\t" \
  "s_waitcnt lgkmcnt(8)\n\t" \
  "v_mfma_f32_16x16x32_bf16 v[144:147], v[96:99], v[180:183], v[144:147]\n\t" \
  "ds_read_b64_tr_b16 v[200:201], v224 offset:46080\n\t" \
  "v_mfma_f32_16x16x32_bf16 v[148:151], v[100:103], v[180:183], v[148:151]\n\t" \
  "ds_read_b64_tr_b16 v[202:203], v224 offset:50688\n\t" \
  "s_waitcnt lgkmcnt(8)\n\t" \
  "v_mfma_f32_16x16x32_bf16 v[152:155], v[96:99], v[184:187], v[152:155]\n\t" \
  "ds_read_b64_tr_b16 v[204:205], v224 offset:46120\n\t" \
  "ds_read_b64_tr_b16 v[206:207], v224 offset:50728\n\t" \
  "v_mfma_f32_16x16x32_bf16 v[156:159], v[100:103], v[184:187], v[156:159]\n\t" \
  "ds_read_b64_tr_b16 v[208:209], v224 offset:46224\n\t" \
  "s_waitcnt lgkmcnt(9)\n\t" \
  "v_mfma_f32_16x16x32_bf16 v[160:163], v[96:99], v[188:191], v[160:163]\n\t" \
  "ds_read_b64_tr_b16 v[210:211], v224 offset:50832\n\t" \
  "v_mfma_f32_16x16x32_bf16 v[164:167], v[100:103], v[188:191], v[164:167]\n\t" \
  "ds_read_b64_tr_b16 v[212:213], v224 offset:46264\n\t" \
  "s_waitcnt lgkmcnt(9)\n\t" \
  "v_mfma_f32_16x16x32_bf16 v[168:171], v[96:99], v[192:195], v[168:171]\n\t" \
  "ds_read_b64_tr_b16 v[214:215], v224 offset:50872\n\t" \
  "v_mfma_f32_16x16x32_bf16 v[172:175], v[100:103], v[192:195], v[172:175]\n\t" \
  "ds_read_b64_tr_b16 v[216:217], v226 offset:46080\n\t" \
  "s_waitcnt lgkmcnt(9)\n\t" \
  "v_mfma_f32_16x16x32_bf16 v[176:179], v[96:99], v[196:199], v[176:179]\n\t" \
  "ds_read_b64_tr_b16 v[218:219], v226 offset:50688\n\t" \
  "s_waitcnt lgkmcnt(8)\n\t" \
  "v_mfma_f32_16x16x32_bf16 v[144:147], v[104:107], v[200:203], v[144:147]\n\t" \
  "ds_read_b64_tr_b16 v[180:181], v225 offset:18432\n\t" \
  "v_mfma_f32_16x16x32_bf16 v[148:151], v[108:111], v[200:203], v[148:151]\n\t" \
  "ds_read_b64_tr_b16 v[182:183], v225 offset:23040\n\t" \
  "s_waitcnt lgkmcnt(8)\n\t" \
  "v_mfma_f32_16x16x32_bf16 v[152:155], v[104:107], v[204:207], v[152:155]\n\t" \
  "ds_read_b64_tr_b16 v[184:185], v225 offset:18472\n\t" \
  "ds_read_b64_tr_b16 v[186:187], v225 offset:23080\n\t" \
  "v_mfma_f32_16x16x32_bf16 v[156:159], v[108:111], v[204:207], v[156:159]\n\t" \
  "ds_read_b64_tr_b16 v[188:189], v225 offset:18576\n\t" \
  "s_waitcnt lgkmcnt(9)\n\t" \
  "v_mfma_f32_16x16x32_bf16 v[160:163], v[104:107], v[208:211], v[160:163]\n\t" \
  "ds_read_b64_tr_b16 v[190:191], v225 offset:23184\n\t" \
  "v_mfma_f32_16x16x32_bf16 v[164:167], v[108:111], v[208:211], v[164:167]\n\t" \
  "ds_read_b64_tr_b16 v[192:193], v225 offset:18616\n\t" \
  "s_waitcnt lgkmcnt(9)\n\t" \
  "v_mfma_f32_16x16x32_bf16 v[168:171], v[104:107], v[212:215], v[168:171]\n\t" \
  "ds_read_b64_tr_b16 v[194:195], v225 offset:23224\n\t" \
  "v_mfma_f32_16x16x32_bf16 v[172:175], v[108:111], v[212:215], v[172:175]\n\t" \
  "ds_read_b64_tr_b16 v[196:197], v227 offset:18432\n\t" \
  "s_waitcnt lgkmcnt(9)\n\t" \
  "v_mfma_f32_16x16x32_bf16 v[176:179], v[104:107], v[216:219], v[176:179]\n\t" \
  "ds_read_b64_tr_b16 v[198:199], v227 offset:23040\n\t" \
  "s_cmp_lg_u32 %[done], 0\n\t" \
  "s_cbranch_scc1 .Lp1f56_%=\n\t" \
  "global_load_dwordx2 %[pk], %[fa], %[fl] sc1\n\t" \
  ".Lp1f56_%=:\n\t" \
  "s_waitcnt lgkmcnt(8)\n\t" \
  "v_mfma_f32_16x16x32_bf16 v[144:147], v[0:3], v[180:183], v[144:147]\n\t" \
  "ds_read_b64_tr_b16 v[200:201], v225 offset:27648\n\t" \
  "v_mfma_f32_16x16x32_bf16 v[148:151], v[4:7], v[180:183], v[148:151]\n\t" \
  "ds_read_b64_tr_b16 v[202:203], v225 offset:32256\n\t" \
  "s_waitcnt lgkmcnt(8)\n\t" \
  "v_mfma_f32_16x16x32_bf16 v[152:155], v[0:3], v[184:187], v[152:155]\n\t" \
  "ds_read_b64_tr_b16 v[204:205], v225 offset:27688\n\t" \
  "ds_read_b64_tr_b16 v[206:207], v225 offset:32296\n\t" \
  "v_mfma_f32_16x16x32_bf16 v[156:159], v[4:7], v[184:187], v[156:159]\n\t" \
  "ds_read_b64_tr_b16 v[208:209], v225 offset:27792\n\t" \
  "s_waitcnt lgkmcnt(9)\n\t" \
  "v_mfma_f32_16x16x32_bf16 v[160:163], v[0:3], v[188:191], v[160:163]\n\t" \
  "ds_read_b64_tr_b16 v[210:211], v225 offset:32400\n\t" \
  "v_mfma_f32_16x16x32_bf16 v[164:167], v[4:7], v[188:191], v[164:167]\n\t" \
  "ds_read_b64_tr_b16 v[212:213], v225 offset:27832\n\t" \
  "s_waitcnt lgkmcnt(9)\n\t" \
  "v_mfma_f32_16x16x32_bf16 v[168:171], v[0:3], v[192:195], v[168:171]\n\t" \
  "ds_read_b64_tr_b16 v[214:215], v225 offset:32440\n\t" \
  "v_mfma_f32_16x16x32_bf16 v[172:175], v[4:7], v[192:195], v[172:175]\n\t" \
  "ds_read_b64_tr_b16 v[216:217], v227 offset:27648\n\t" \
  "s_waitcnt lgkmcnt(9)\n\t" \
  "v_mfma_f32_16x16x32_bf16 v[176:179], v[0:3], v[196:199], v[176:179]\n\t" \
  "ds_read_b64_tr_b16 v[218:219], v227 offset:32256\n\t" \
  "s_waitcnt lgkmcnt(8)\n\t" \
  "v_mfma_f32_16x16x32_bf16 v[144:147], v[8:11], v[200:203], v[144:147]\n\t" \
  "ds_read_b64_tr_b16 v[180:181], v225 offset:0\n\t" \
  "v_mfma_f32_16x16x32_bf16 v[148:151], v[12:15], v[200:203], v[148:151]\n\t" \
  "ds_read_b64_tr_b16 v[182:183], v225 offset:4608\n\t" \
  "s_waitcnt lgkmcnt(8)\n\t" \
  "v_mfma_f32_16x16x32_bf16 v[152:155], v[8:11], v[204:207], v[152:155]\n\t" \
  "ds_read_b64_tr_b16 v[184:185], v225 offset:40\n\t" \
  "ds_read_b64_tr_b16 v[186:187], v225 offset:4648\n\t" \
  "v_mfma_f32_16x16x32_bf16 v[156:159], v[12:15], v[204:207], v[156:159]\n\t" \
  "ds_read_b64_tr_b16 v[188:189], v225 offset:144\n\t" \
  "s_waitcnt lgkmcnt(9)\n\t" \
  "v_mfma_f32_16x16x32_bf16 v[160:163], v[8:11], v[208:211], v[160:163]\n\t" \
  "ds_read_b64_tr_b16 v[190:191], v225 offset:4752\n\t" \
  "v_mfma_f32_16x16x32_bf16 v[164:167], v[12:15], v[208:211], v[164:167]\n\t" \
  "ds_read_b64_tr_b16 v[192:193], v225 offset:184\n\t" \
  "s_waitcnt lgkmcnt(9)\n\t" \
  "v_mfma_f32_16x16x32_bf16 v[168:171], v[8:11], v[212:215], v[168:171]\n\t" \
  "ds_read_b64_tr_b16 v[194:195], v225 offset:4792\n\t" \
  "v_mfma_f32_16x16x32_bf16 v[172:175], v[12:15], v[212:215], v[172:175]\n\t" \
  "ds_read_b64_tr_b16 v[196:197], v227 offset:0\n\t" \
  "s_waitcnt lgkmcnt(9)\n\t" \
  "v_mfma_f32_16x16x32_bf16 v[176:179], v[8:11], v[216:219], v[176:179]\n\t" \
  "ds_read_b64_tr_b16 v[198:199], v227 offset:4608\n\t" \
  "s_cmp_lg_u32 %[done], 0\n\t" \
  "s_cbranch_scc1 .Lp1f58_%=\n\t" \
  "s_waitcnt vmcnt(0)\n\t" \
  "v_cmp_ne_u64 vcc, %[pk], %[tag]\n\t" \
  "s_cbranch_vccnz .Lp1f57_%=\n\t" \
  "s_mov_b32 %[done], 1\n\t" \
  "s_add_u32 m0, %[m1], 150528\n\t" \
  "s_add_u32 %[sw], %[sq], 0\n\t" \
  "buffer_load_dwordx4 %[l16], %[rw], %[sw] offen sc1 lds\n\t" \
  "s_mov_b32 m0, %[m7]\n\t" \
  "s_nop 0\n\t" \
  "buffer_load_dwordx4 %[l16], %[rw], %[s7] offen sc1 lds\n\t" \
  "s_add_u32 m0, %[m1], 0\n\t" \
  "s_add_u32 %[sw], %[sk], 0\n\t" \
  "buffer_load_dwordx4 %[l16], %[rw], %[sw] offen sc1 lds\n\t" \
  "s_add_u32 m0, %[m1], 7168\n\t" \
  "s_add_u32 %[sw], %[sk], 7168\n\t" \
  "buffer_load_dwordx4 %[l16], %[rw], %[sw] offen sc1 lds\n\t" \
  "s_add_u32 m0, %[m1], 14336\n\t" \
  "s_add_u32 %[sw], %[sk], 14336\n\t" \
  "buffer_load_dwordx4 %[l16], %[rw], %[sw] offen sc1 lds\n\t" \
  "s_add_u32 m0, %[m1], 21504\n\t" \
  "s_add_u32 %[sw], %[sk], 21504\n\t" \
  "buffer_load_dwordx4 %[l16], %[rw], %[sw] offen sc1 lds\n\t" \
  "s_add_u32 m0, %[m1], 28672\n\t" \
  "s_add_u32 %[sw], %[sk], 28672\n\t" \
  "buffer_load_dwordx4 %[l16], %[rw], %[sw] offen sc1 lds\n\t" \
  "s_add_u32 m0, %[m1], 35840\n\t" \
  "s_add_u32 %[sw], %[sk], 35840\n\t" \
  "buffer_load_dwordx4 %[l16], %[rw], %[sw] offen sc1 lds\n\t" \
  "s_branch .Lp1f58_%=\n\t" \
  ".Lp1f57_%=:\n\t" \
  "global_load_dwordx2 %[pk], %[fa], %[fl] sc1\n\t" \
  ".Lp1f60_%=:\n\t" \
  ".Lp1f58_%=:\n\t" \
  "s_waitcnt lgkmcnt(8)\n\t" \
  "v_mfma_f32_16x16x32_bf16 v[144:147], v[16:19], v[180:183], v[144:147]\n\t" \
  "ds_read_b64_tr_b16 v[200:201], v225 offset:9216\n\t" \
  "v_mfma_f32_16x16x32_bf16 v[148:151], v[20:23], v[180:183], v[148:151]\n\t" \
  "ds_read_b64_tr_b16 v[202:203], v225 offset:13824\n\t" \
  "s_waitcnt lgkmcnt(8)\n\t" \
  "v_mfma_f32_16x16x32_bf16 v[152:155], v[16:19], v[184:187], v[152:155]\n\t" \
  "ds_read_b64_tr_b16 v[204:205], v225 offset:9256\n\t" \
  "ds_read_b64_tr_b16 v[206:207], v225 offset:13864\n\t" \
  "v_mfma_f32_16x16x32_bf16 v[156:159], v[20:23], v[184:187], v[156:159]\n\t" \
  "ds_read_b64_tr_b16 v[208:209], v225 offset:9360\n\t" \
  "s_waitcnt lgkmcnt(9)\n\t" \
  "v_mfma_f32_16x16x32_bf16 v[160:163], v[16:19], v[188:191], v[160:163]\n\t" \
  "ds_read_b64_tr_b16 v[210:211], v225 offset:13968\n\t" \
  "v_mfma_f32_16x16x32_bf16 v[164:167], v[20:23], v[188:191], v[164:167]\n\t" \
  "ds_read_b64_tr_b16 v[212:213], v225 offset:9400\n\t" \
  "s_waitcnt lgkmcnt(9)\n\t" \
  "v_mfma_f32_16x16x32_bf16 v[168:171], v[16:19], v[192:195], v[168:171]\n\t" \
  "ds_read_b64_tr_b16 v[214:215], v225 offset:14008\n\t" \
  "v_mfma_f32_16x16x32_bf16 v[172:175], v[20:23], v[192:195], v[172:175]\n\t" \
  "ds_read_b64_tr_b16 v[216:217], v227 offset:9216\n\t" \
  "s_waitcnt lgkmcnt(9)\n\t" \
  "v_mfma_f32_16x16x32_bf16 v[176:179], v[16:19], v[196:199], v[176:179]\n\t" \
  "ds_read_b64_tr_b16 v[218:219], v227 offset:13824\n\t" \
  "s_waitcnt lgkmcnt(8)\n\t" \
  "v_mfma_f32_16x16x32_bf16 v[144:147], v[24:27], v[200:203], v[144:147]\n\t" \
  "v_mfma_f32_16x16x32_bf16 v[148:151], v[28:31], v[200:203], v[148:151]\n\t" \
  "s_waitcnt lgkmcnt(6)\n\t" \
  "v_mfma_f32_16x16x32_bf16 v[152:155], v[24:27], v[204:207], v[152:155]\n\t" \
  "v_mfma_f32_16x16x32_bf16 v[156:159], v[28:31], v[204:207], v[156:159]\n\t" \
  "s_waitcnt lgkmcnt(4)\n\t" \
  "v_mfma_f32_16x16x32_bf16 v[160:163], v[24:27], v[208:211], v[160:163]\n\t" \
  "v_mfma_f32_16x16x32_bf16 v[164:167], v[28:31], v[208:211], v[164:167]\n\t" \
  "s_waitcnt lgkmcnt(2)\n\t" \
  "v_mfma_f32_16x16x32_bf16 v[168:171], v[24:27], v[212:215], v[168:171]\n\t" \
  "v_mfma_f32_16x16x32_bf16 v[172:175], v[28:31], v[212:215], v[172:175]\n\t" \
  "s_waitcnt lgkmcnt(0)\n\t" \
  "v_mfma_f32_16x16x32_bf16 v[176:179], v[24:27], v[216:219], v[176:179]\n\t" \
  "s_waitcnt lgkmcnt(0)\n\t" \
  "s_nop 7\n\t" \
  "s_cmp_lg_u32 %[done], 0\n\t" \
  "s_cbranch_scc1 .Lp1f62_%=\n\t" \
  "s_waitcnt vmcnt(0)\n\t" \
  "v_cmp_ne_u64 vcc, %[pk], %[tag]\n\t" \
  "s_cbranch_vccnz .Lp1f61_%=\n\t" \
  "s_mov_b32 %[done], 1\n\t" \
  "s_add_u32 m0, %[m1], 150528\n\t" \
  "s_add_u32 %[sw], %[sq], 0\n\t" \
  "buffer_load_dwordx4 %[l16], %[rw], %[sw] offen sc1 lds\n\t" \
  "s_mov_b32 m0, %[m7]\n\t" \
  "s_nop 0\n\t" \
  "buffer_load_dwordx4 %[l16], %[rw], %[s7] offen sc1 lds\n\t" \
  "s_add_u32 m0, %[m1], 0\n\t" \
  "s_add_u32 %[sw], %[sk], 0\n\t" \
  "buffer_load_dwordx4 %[l16], %[rw], %[sw] offen sc1 lds\n\t" \
  "s_add_u32 m0, %[m1], 7168\n\t" \
  "s_add_u32 %[sw], %[sk], 7168\n\t" \
  "buffer_load_dwordx4 %[l16], %[rw], %[sw] offen sc1 lds\n\t" \
  "s_add_u32 m0, %[m1], 14336\n\t" \
  "s_add_u32 %[sw], %[sk], 14336\n\t" \
  "buffer_load_dwordx4 %[l16], %[rw], %[sw] offen sc1 lds\n\t" \
  "s_add_u32 m0, %[m1], 21504\n\t" \
  "s_add_u32 %[sw], %[sk], 21504\n\t" \
  "buffer_load_dwordx4 %[l16], %[rw], %[sw] offen sc1 lds\n\t" \
  "s_add_u32 m0, %[m1], 28672\n\t" \
  "s_add_u32 %[sw], %[sk], 28672\n\t" \
  "buffer_load_dwordx4 %[l16], %[rw], %[sw] offen sc1 lds\n\t" \
  "s_add_u32 m0, %[m1], 35840\n\t" \
  "s_add_u32 %[sw], %[sk], 35840\n\t" \
  "buffer_load_dwordx4 %[l16], %[rw], %[sw] offen sc1 lds\n\t" \
  "s_branch .Lp1f62_%=\n\t" \
  ".Lp1f61_%=:\n\t" \
  ".Lp1f62_%=:\n\t"

#define P3_RDP \
  "ds_read_b64_tr_b16 v[0:1], %[bm] offset:0\n\t" \
  "ds_read_b64_tr_b16 v[2:3], %[bm] offset:144\n\t" \
  "ds_read_b64_tr_b16 v[4:5], %[bm] offset:32\n\t" \
  "ds_read_b64_tr_b16 v[6:7], %[bm] offset:176\n\t" \
  "ds_read_b64_tr_b16 v[8:9], %[bm] offset:64\n\t" \
  "ds_read_b64_tr_b16 v[10:11], %[bm] offset:208\n\t" \
  "ds_read_b64_tr_b16 v[12:13], %[bm] offset:96\n\t" \
  "ds_read_b64_tr_b16 v[14:15], %[bm] offset:240\n\t" \
  "ds_read_b64_tr_b16 v[16:17], %[bm] offset:128\n\t" \
  "ds_read_b64_tr_b16 v[18:19], %[bm] offset:272\n\t" \
  "s_sleep 1\n\t" \
  "ds_read_b64_tr_b16 v[20:21], %[bm] offset:5184\n\t" \
  "ds_read_b64_tr_b16 v[22:23], %[bm] offset:5328\n\t" \
  "ds_read_b64_tr_b16 v[24:25], %[bm] offset:5216\n\t" \
  "ds_read_b64_tr_b16 v[26:27], %[bm] offset:5360\n\t" \
  "ds_read_b64_tr_b16 v[28:29], %[bm] offset:5248\n\t" \
  "ds_read_b64_tr_b16 v[30:31], %[bm] offset:5392\n\t" \
  "ds_read_b64_tr_b16 v[32:33], %[bm] offset:5280\n\t" \
  "ds_read_b64_tr_b16 v[34:35], %[bm] offset:5424\n\t" \
  "ds_read_b64_tr_b16 v[36:37], %[bm] offset:5312\n\t" \
  "ds_read_b64_tr_b16 v[38:39], %[bm] offset:5456\n\t" \
  "s_sleep 1\n\t" \
  "ds_read_b64_tr_b16 v[40:41], %[bm] offset:10368\n\t" \
  "ds_read_b64_tr_b16 v[42:43], %[bm] offset:10512\n\t" \
  "ds_read_b64_tr_b16 v[44:45], %[bm] offset:10400\n\t" \
  "ds_read_b64_tr_b16 v[46:47], %[bm] offset:10544\n\t" \
  "ds_read_b64_tr_b16 v[48:49], %[bm] offset:10432\n\t" \
  "ds_read_b64_tr_b16 v[50:51], %[bm] offset:10576\n\t" \
  "ds_read_b64_tr_b16 v[52:53], %[bm] offset:10464\n\t" \
  "ds_read_b64_tr_b16 v[54:55], %[bm] offset:10608\n\t" \
  "ds_read_b64_tr_b16 v[56:57], %[bm] offset:10496\n\t" \
  "ds_read_b64_tr_b16 v[58:59], %[bm] offset:10640\n\t" \
  "s_sleep 1\n\t" \
  "ds_read_b64_tr_b16 v[60:61], %[bm] offset:15552\n\t" \
  "ds_read_b64_tr_b16 v[62:63], %[bm] offset:15696\n\t" \
  "ds_read_b64_tr_b16 v[64:65], %[bm] offset:15584\n\t" \
  "ds_read_b64_tr_b16 v[66:67], %[bm] offset:15728\n\t" \
  "ds_read_b64_tr_b16 v[68:69], %[bm] offset:15616\n\t" \
  "ds_read_b64_tr_b16 v[70:71], %[bm] offset:15760\n\t" \
  "ds_read_b64_tr_b16 v[72:73], %[bm] offset:15648\n\t" \
  "ds_read_b64_tr_b16 v[74:75], %[bm] offset:15792\n\t" \
  "ds_read_b64_tr_b16 v[76:77], %[bm] offset:15680\n\t" \
  "ds_read_b64_tr_b16 v[78:79], %[bm] offset:15824\n\t" \
  "s_sleep 1\n\t" \
  "ds_read_b64_tr_b16 v[80:81], %[bm] offset:20736\n\t" \
  "ds_read_b64_tr_b16 v[82:83], %[bm] offset:20880\n\t" \
  "ds_read_b64_tr_b16 v[84:85], %[bm] offset:20768\n\t" \
  "ds_read_b64_tr_b16 v[86:87], %[bm] offset:20912\n\t" \
  "ds_read_b64_tr_b16 v[88:89], %[bm] offset:20800\n\t" \
  "ds_read_b64_tr_b16 v[90:91], %[bm] offset:20944\n\t" \
  "ds_read_b64_tr_b16 v[92:93], %[bm] offset:20832\n\t" \
  "ds_read_b64_tr_b16 v[94:95], %[bm] offset:20976\n\t" \
  "ds_read_b64_tr_b16 v[96:97], %[bm] offset:20864\n\t" \
  "ds_read_b64_tr_b16 v[98:99], %[bm] offset:21008\n\t" \
  "s_sleep 1\n\t" \
  "ds_read_b64_tr_b16 v[100:101], %[bm] offset:25920\n\t" \
  "ds_read_b64_tr_b16 v[102:103], %[bm] offset:26064\n\t" \
  "ds_read_b64_tr_b16 v[104:105], %[bm] offset:25952\n\t" \
  "ds_read_b64_tr_b16 v[106:107], %[bm] offset:26096\n\t" \
  "ds_read_b64_tr_b16 v[108:109], %[bm] offset:25984\n\t" \
  "ds_read_b64_tr_b16 v[110:111], %[bm] offset:26128\n\t" \
  "ds_read_b64_tr_b16 v[112:113], %[bm] offset:26016\n\t" \
  "ds_read_b64_tr_b16 v[114:115], %[bm] offset:26160\n\t" \
  "ds_read_b64_tr_b16 v[116:117], %[bm] offset:26048\n\t" \
  "ds_read_b64_tr_b16 v[118:119], %[bm] offset:26192\n\t" \
  "s_sleep 1\n\t" \
  "ds_read_b64_tr_b16 v[120:121], %[bm] offset:31104\n\t" \
  "ds_read_b64_tr_b16 v[122:123], %[bm] offset:31248\n\t" \
  "ds_read_b64_tr_b16 v[124:125], %[bm] offset:31136\n\t" \
  "ds_read_b64_tr_b16 v[126:127], %[bm] offset:31280\n\t" \
  "ds_read_b64_tr_b16 v[128:129], %[bm] offset:31168\n\t" \
  "ds_read_b64_tr_b16 v[130:131], %[bm] offset:31312\n\t" \
  "ds_read_b64_tr_b16 v[132:133], %[bm] offset:31200\n\t" \
  "ds_read_b64_tr_b16 v[134:135], %[bm] offset:31344\n\t" \
  "ds_read_b64_tr_b16 v[136:137], %[bm] offset:31232\n\t" \
  "ds_read_b64_tr_b16 v[138:139], %[bm] offset:31376\n\t" \
  "s_sleep 1\n\t" \
  "ds_read_b64_tr_b16 v[140:141], %[bm] offset:36288\n\t" \
  "ds_read_b64_tr_b16 v[142:143], %[bm] offset:36432\n\t" \
  "ds_read_b64_tr_b16 v[144:145], %[bm] offset:36320\n\t" \
  "ds_read_b64_tr_b16 v[146:147], %[bm] offset:36464\n\t" \
  "ds_read_b64_tr_b16 v[148:149], %[bm] offset:36352\n\t" \
  "ds_read_b64_tr_b16 v[150:151], %[bm] offset:36496\n\t" \
  "ds_read_b64_tr_b16 v[152:153], %[bm] offset:36384\n\t" \
  "ds_read_b64_tr_b16 v[154:155], %[bm] offset:36528\n\t" \
  "ds_read_b64_tr_b16 v[156:157], %[bm] offset:36416\n\t" \
  "ds_read_b64_tr_b16 v[158:159], %[bm] offset:36560\n\t" \
  "s_sleep 1\n\t" \
  "ds_read_b64_tr_b16 v[160:161], %[br] offset:0\n\t" \
  "ds_read_b64_tr_b16 v[162:163], %[br] offset:5184\n\t" \
  "ds_read_b64_tr_b16 v[164:165], %[br] offset:32\n\t" \
  "ds_read_b64_tr_b16 v[166:167], %[br] offset:5216\n\t" \
  "ds_read_b64_tr_b16 v[168:169], %[br] offset:64\n\t" \
  "ds_read_b64_tr_b16 v[170:171], %[br] offset:5248\n\t" \
  "ds_read_b64_tr_b16 v[172:173], %[br] offset:96\n\t" \
  "ds_read_b64_tr_b16 v[174:175], %[br] offset:5280\n\t" \
  "ds_read_b64_tr_b16 v[176:177], %[br] offset:128\n\t" \
  "ds_read_b64_tr_b16 v[178:179], %[br] offset:5312\n\t" \
  "s_sleep 1\n\t" \
  "s_waitcnt lgkmcnt(0)\n\t"
#define P3_LD \
  "s_add_u32 %[t], %[so], 0\n\t" \
  "buffer_load_dwordx4 v[180:183], %[mo], %[ws], %[t] offen sc1\n\t" \
  "s_add_u32 %[t], %[so], 147456\n\t" \
  "buffer_load_dwordx4 v[184:187], %[mo], %[ws], %[t] offen sc1\n\t" \
  "s_add_u32 %[t], %[so], 294912\n\t" \
  "buffer_load_dwordx4 v[188:191], %[mo], %[ws], %[t] offen sc1\n\t" \
  "s_add_u32 %[t], %[so], 442368\n\t" \
  "buffer_load_dwordx4 v[192:195], %[mo], %[ws], %[t] offen sc1\n\t" \
  "s_add_u32 %[t], %[so], 589824\n\t" \
  "buffer_load_dwordx4 v[196:199], %[mo], %[ws], %[t] offen sc1\n\t" \
  "s_add_u32 %[t], %[so], 737280\n\t" \
  "buffer_load_dwordx4 v[200:203], %[mo], %[ws], %[t] offen sc1\n\t" \
  "s_add_u32 %[t], %[so], 884736\n\t" \
  "buffer_load_dwordx4 v[204:207], %[mo], %[ws], %[t] offen sc1\n\t" \
  "s_add_u32 %[t], %[so], 1032192\n\t" \
  "buffer_load_dwordx4 v[208:211], %[mo], %[ws], %[t] offen sc1\n\t" \
  "s_add_u32 %[t], %[so], 0\n\t" \
  "buffer_load_dword v212, %[ro], %[ws], %[t] offen sc1\n\t" \
  "s_add_u32 %[t], %[so], 73728\n\t" \
  "buffer_load_dword v213, %[ro], %[ws], %[t] offen sc1\n\t" \
  "s_add_u32 %[t], %[so], 147456\n\t" \
  "buffer_load_dword v214, %[ro], %[ws], %[t] offen sc1\n\t" \
  "s_add_u32 %[t], %[so], 221184\n\t" \
  "buffer_load_dword v215, %[ro], %[ws], %[t] offen sc1\n\t" \
  "s_waitcnt vmcnt(0)\n\t"
#define P3_ST \
  "s_add_u32 m0, %[sm], 0\n\t" \
  "s_add_u32 %[t], %[so], 0\n\t" \
  "buffer_load_dwordx4 %[mo], %[ws], %[t] offen sc1 lds\n\t" \
  "s_add_u32 m0, %[sm], 1024\n\t" \
  "s_add_u32 %[t], %[so], 147456\n\t" \
  "buffer_load_dwordx4 %[mo], %[ws], %[t] offen sc1 lds\n\t" \
  "s_add_u32 m0, %[sm], 2048\n\t" \
  "s_add_u32 %[t], %[so], 294912\n\t" \
  "buffer_load_dwordx4 %[mo], %[ws], %[t] offen sc1 lds\n\t" \
  "s_add_u32 m0, %[sm], 3072\n\t" \
  "s_add_u32 %[t], %[so], 442368\n\t" \
  "buffer_load_dwordx4 %[mo], %[ws], %[t] offen sc1 lds\n\t" \
  "s_add_u32 m0, %[sm], 4096\n\t" \
  "s_add_u32 %[t], %[so], 589824\n\t" \
  "buffer_load_dwordx4 %[mo], %[ws], %[t] offen sc1 lds\n\t" \
  "s_add_u32 m0, %[sm], 5120\n\t" \
  "s_add_u32 %[t], %[so], 737280\n\t" \
  "buffer_load_dwordx4 %[mo], %[ws], %[t] offen sc1 lds\n\t" \
  "s_add_u32 m0, %[sm], 6144\n\t" \
  "s_add_u32 %[t], %[so], 884736\n\t" \
  "buffer_load_dwordx4 %[mo], %[ws], %[t] offen sc1 lds\n\t" \
  "s_add_u32 m0, %[sm], 7168\n\t" \
  "s_add_u32 %[t], %[so], 1032192\n\t" \
  "buffer_load_dwordx4 %[mo], %[ws], %[t] offen sc1 lds\n\t" \
  "s_add_u32 m0, %[sm], 8192\n\t" \
  "s_add_u32 %[t], %[so], 0\n\t" \
  "buffer_load_dword %[ro], %[ws], %[t] offen sc1 lds\n\t" \
  "s_add_u32 m0, %[sm], 8448\n\t" \
  "s_add_u32 %[t], %[so], 73728\n\t" \
  "buffer_load_dword %[ro], %[ws], %[t] offen sc1 lds\n\t" \
  "s_add_u32 m0, %[sm], 8704\n\t" \
  "s_add_u32 %[t], %[so], 147456\n\t" \
  "buffer_load_dword %[ro], %[ws], %[t] offen sc1 lds\n\t" \
  "s_add_u32 m0, %[sm], 8960\n\t" \
  "s_add_u32 %[t], %[so], 221184\n\t" \
  "buffer_load_dword %[ro], %[ws], %[t] offen sc1 lds\n\t" \
  "s_waitcnt vmcnt(0)\n\t"
#define P3_BB \
  "v_mfma_f32_16x16x32_bf16 v[216:219], v[0:3], v[180:183], 0\n\t" \
  "v_mfma_f32_16x16x32_bf16 v[220:223], v[4:7], v[180:183], 0\n\t" \
  "v_mfma_f32_16x16x32_bf16 v[224:227], v[8:11], v[180:183], 0\n\t" \
  "v_mfma_f32_16x16x32_bf16 v[228:231], v[12:15], v[180:183], 0\n\t" \
  "v_mfma_f32_16x16x32_bf16 v[232:235], v[16:19], v[180:183], 0\n\t" \
  "ds_read_b128 v[180:183], %[st] offset:0\n\t" \
  "v_mfma_f32_16x16x32_bf16 v[216:219], v[20:23], v[184:187], v[216:219]\n\t" \
  "v_mfma_f32_16x16x32_bf16 v[220:223], v[24:27], v[184:187], v[220:223]\n\t" \
  "v_mfma_f32_16x16x32_bf16 v[224:227], v[28:31], v[184:187], v[224:227]\n\t" \
  "v_mfma_f32_16x16x32_bf16 v[228:231], v[32:35], v[184:187], v[228:231]\n\t" \
  "v_mfma_f32_16x16x32_bf16 v[232:235], v[36:39], v[184:187], v[232:235]\n\t" \
  "ds_read_b128 v[184:187], %[st] offset:1024\n\t" \
  "v_mfma_f32_16x16x32_bf16 v[216:219], v[40:43], v[188:191], v[216:219]\n\t" \
  "v_mfma_f32_16x16x32_bf16 v[220:223], v[44:47], v[188:191], v[220:223]\n\t" \
  "v_mfma_f32_16x16x32_bf16 v[224:227], v[48:51], v[188:191], v[224:227]\n\t" \
  "v_mfma_f32_16x16x32_bf16 v[228:231], v[52:55], v[188:191], v[228:231]\n\t" \
  "v_mfma_f32_16x16x32_bf16 v[232:235], v[56:59], v[188:191], v[232:235]\n\t" \
  "ds_read_b128 v[188:191], %[st] offset:2048\n\t" \
  "v_mfma_f32_16x16x32_bf16 v[216:219], v[60:63], v[192:195], v[216:219]\n\t" \
  "v_mfma_f32_16x16x32_bf16 v[220:223], v[64:67], v[192:195], v[220:223]\n\t" \
  "v_mfma_f32_16x16x32_bf16 v[224:227], v[68:71], v[192:195], v[224:227]\n\t" \
  "v_mfma_f32_16x16x32_bf16 v[228:231], v[72:75], v[192:195], v[228:231]\n\t" \
  "v_mfma_f32_16x16x32_bf16 v[232:235], v[76:79], v[192:195], v[232:235]\n\t" \
  "ds_read_b128 v[192:195], %[st] offset:3072\n\t" \
  "v_mfma_f32_16x16x32_bf16 v[216:219], v[80:83], v[196:199], v[216:219]\n\t" \
  "v_mfma_f32_16x16x32_bf16 v[220:223], v[84:87], v[196:199], v[220:223]\n\t" \
  "v_mfma_f32_16x16x32_bf16 v[224:227], v[88:91], v[196:199], v[224:227]\n\t" \
  "v_mfma_f32_16x16x32_bf16 v[228:231], v[92:95], v[196:199], v[228:231]\n\t" \
  "v_mfma_f32_16x16x32_bf16 v[232:235], v[96:99], v[196:199], v[232:235]\n\t" \
  "ds_read_b128 v[196:199], %[st] offset:4096\n\t" \
  "v_mfma_f32_16x16x32_bf16 v[216:219], v[100:103], v[200:203], v[216:219]\n\t" \
  "v_mfma_f32_16x16x32_bf16 v[220:223], v[104:107], v[200:203], v[220:223]\n\t" \
  "v_mfma_f32_16x16x32_bf16 v[224:227], v[108:111], v[200:203], v[224:227]\n\t" \
  "v_mfma_f32_16x16x32_bf16 v[228:231], v[112:115], v[200:203], v[228:231]\n\t" \
  "v_mfma_f32_16x16x32_bf16 v[232:235], v[116:119], v[200:203], v[232:235]\n\t" \
  "ds_read_b128 v[200:203], %[st] offset:5120\n\t" \
  "v_mfma_f32_16x16x32_bf16 v[216:219], v[120:123], v[204:207], v[216:219]\n\t" \
  "v_mfma_f32_16x16x32_bf16 v[220:223], v[124:127], v[204:207], v[220:223]\n\t" \
  "v_mfma_f32_16x16x32_bf16 v[224:227], v[128:131], v[204:207], v[224:227]\n\t" \
  "v_mfma_f32_16x16x32_bf16 v[228:231], v[132:135], v[204:207], v[228:231]\n\t" \
  "v_mfma_f32_16x16x32_bf16 v[232:235], v[136:139], v[204:207], v[232:235]\n\t" \
  "ds_read_b128 v[204:207], %[st] offset:6144\n\t" \
  "v_mfma_f32_16x16x32_bf16 v[216:219], v[140:143], v[208:211], v[216:219]\n\t" \
  "v_mfma_f32_16x16x32_bf16 v[220:223], v[144:147], v[208:211], v[220:223]\n\t" \
  "v_mfma_f32_16x16x32_bf16 v[224:227], v[148:151], v[208:211], v[224:227]\n\t" \
  "v_mfma_f32_16x16x32_bf16 v[228:231], v[152:155], v[208:211], v[228:231]\n\t" \
  "v_mfma_f32_16x16x32_bf16 v[232:235], v[156:159], v[208:211], v[232:235]\n\t" \
  "ds_read_b128 v[208:211], %[st] offset:7168\n\t" \
  "v_mfma_f32_16x16x32_bf16 v[216:219], v[160:163], v[212:215], v[216:219]\n\t" \
  "v_mfma_f32_16x16x32_bf16 v[220:223], v[164:167], v[212:215], v[220:223]\n\t" \
  "v_mfma_f32_16x16x32_bf16 v[224:227], v[168:171], v[212:215], v[224:227]\n\t" \
  "v_mfma_f32_16x16x32_bf16 v[228:231], v[172:175], v[212:215], v[228:231]\n\t" \
  "v_mfma_f32_16x16x32_bf16 v[232:235], v[176:179], v[212:215], v[232:235]\n\t" \
  "ds_read2st64_b32 v[212:213], %[s4] offset0:0 offset1:1\n\t" \
  "ds_read2st64_b32 v[214:215], %[s4] offset0:2 offset1:3\n\t" \
  "s_nop 7\n\t" \
  "ds_write_b128 %[xa], v[216:219] offset:0\n\t" \
  "ds_write_b128 %[xa], v[220:223] offset:1024\n\t" \
  "ds_write_b128 %[xa], v[224:227] offset:2048\n\t" \
  "ds_write_b128 %[xa], v[228:231] offset:3072\n\t" \
  "ds_write_b128 %[xa], v[232:235] offset:4096\n\t" \
  "s_waitcnt lgkmcnt(0)\n\t" \
  "ds_add_u32 %[sa], %[one] offset:24\n\t" \
  "v_mfma_f32_16x16x32_bf16 v[216:219], v[0:3], v[180:183], 0\n\t" \
  "ds_read_b64_tr_b16 v[0:1], %[bm] offset:0\n\t" \
  "ds_read_b64_tr_b16 v[2:3], %[bm] offset:144\n\t" \
  "v_mfma_f32_16x16x32_bf16 v[220:223], v[4:7], v[180:183], 0\n\t" \
  "ds_read_b64_tr_b16 v[4:5], %[bm] offset:32\n\t" \
  "ds_read_b64_tr_b16 v[6:7], %[bm] offset:176\n\t" \
  "v_mfma_f32_16x16x32_bf16 v[224:227], v[8:11], v[180:183], 0\n\t" \
  "ds_read_b64_tr_b16 v[8:9], %[bm] offset:64\n\t" \
  "ds_read_b64_tr_b16 v[10:11], %[bm] offset:208\n\t" \
  "v_mfma_f32_16x16x32_bf16 v[228:231], v[12:15], v[180:183], 0\n\t" \
  "ds_read_b64_tr_b16 v[12:13], %[bm] offset:96\n\t" \
  "ds_read_b64_tr_b16 v[14:15], %[bm] offset:240\n\t" \
  "v_mfma_f32_16x16x32_bf16 v[232:235], v[16:19], v[180:183], 0\n\t" \
  "ds_read_b64_tr_b16 v[16:17], %[bm] offset:128\n\t" \
  "ds_read_b64_tr_b16 v[18:19], %[bm] offset:272\n\t" \
  "s_add_u32 %[t], %[so], 0\n\t" \
  "buffer_load_dwordx4 v[180:183], %[mo], %[ws], %[t] offen sc1\n\t" \
  "v_mfma_f32_16x16x32_bf16 v[216:219], v[20:23], v[184:187], v[216:219]\n\t" \
  "ds_read_b64_tr_b16 v[20:21], %[bm] offset:5184\n\t" \
  "ds_read_b64_tr_b16 v[22:23], %[bm] offset:5328\n\t" \
  "v_mfma_f32_16x16x32_bf16 v[220:223], v[24:27], v[184:187], v[220:223]\n\t" \
  "ds_read_b64_tr_b16 v[24:25], %[bm] offset:5216\n\t" \
  "ds_read_b64_tr_b16 v[26:27], %[bm] offset:5360\n\t" \
  "v_mfma_f32_16x16x32_bf16 v[224:227], v[28:31], v[184:187], v[224:227]\n\t" \
  "ds_read_b64_tr_b16 v[28:29], %[bm] offset:5248\n\t" \
  "ds_read_b64_tr_b16 v[30:31], %[bm] offset:5392\n\t" \
  "v_mfma_f32_16x16x32_bf16 v[228:231], v[32:35], v[184:187], v[228:231]\n\t" \
  "ds_read_b64_tr_b16 v[32:33], %[bm] offset:5280\n\t" \
  "ds_read_b64_tr_b16 v[34:35], %[bm] offset:5424\n\t" \
  "v_mfma_f32_16x16x32_bf16 v[232:235], v[36:39], v[184:187], v[232:235]\n\t" \
  "ds_read_b64_tr_b16 v[36:37], %[bm] offset:5312\n\t" \
  "ds_read_b64_tr_b16 v[38:39], %[bm] offset:5456\n\t" \
  "s_add_u32 %[t], %[so], 147456\n\t" \
  "buffer_load_dwordx4 v[184:187], %[mo], %[ws], %[t] offen sc1\n\t" \
  "v_mfma_f32_16x16x32_bf16 v[216:219], v[40:43], v[188:191], v[216:219]\n\t" \
  "ds_read_b64_tr_b16 v[40:41], %[bm] offset:10368\n\t" \
  "ds_read_b64_tr_b16 v[42:43], %[bm] offset:10512\n\t" \
  "v_mfma_f32_16x16x32_bf16 v[220:223], v[44:47], v[188:191], v[220:223]\n\t" \
  "ds_read_b64_tr_b16 v[44:45], %[bm] offset:10400\n\t" \
  "ds_read_b64_tr_b16 v[46:47], %[bm] offset:10544\n\t" \
  "v_mfma_f32_16x16x32_bf16 v[224:227], v[48:51], v[188:191], v[224:227]\n\t" \
  "ds_read_b64_tr_b16 v[48:49], %[bm] offset:10432\n\t" \
  "ds_read_b64_tr_b16 v[50:51], %[bm] offset:10576\n\t" \
  "v_mfma_f32_16x16x32_bf16 v[228:231], v[52:55], v[188:191], v[228:231]\n\t" \
  "ds_read_b64_tr_b16 v[52:53], %[bm] offset:10464\n\t" \
  "ds_read_b64_tr_b16 v[54:55], %[bm] offset:10608\n\t" \
  "v_mfma_f32_16x16x32_bf16 v[232:235], v[56:59], v[188:191], v[232:235]\n\t" \
  "ds_read_b64_tr_b16 v[56:57], %[bm] offset:10496\n\t" \
  "ds_read_b64_tr_b16 v[58:59], %[bm] offset:10640\n\t" \
  "s_add_u32 %[t], %[so], 294912\n\t" \
  "buffer_load_dwordx4 v[188:191], %[mo], %[ws], %[t] offen sc1\n\t" \
  "v_mfma_f32_16x16x32_bf16 v[216:219], v[60:63], v[192:195], v[216:219]\n\t" \
  "ds_read_b64_tr_b16 v[60:61], %[bm] offset:15552\n\t" \
  "ds_read_b64_tr_b16 v[62:63], %[bm] offset:15696\n\t" \
  "v_mfma_f32_16x16x32_bf16 v[220:223], v[64:67], v[192:195], v[220:223]\n\t" \
  "ds_read_b64_tr_b16 v[64:65], %[bm] offset:15584\n\t" \
  "ds_read_b64_tr_b16 v[66:67], %[bm] offset:15728\n\t" \
  "v_mfma_f32_16x16x32_bf16 v[224:227], v[68:71], v[192:195], v[224:227]\n\t" \
  "ds_read_b64_tr_b16 v[68:69], %[bm] offset:15616\n\t" \
  "ds_read_b64_tr_b16 v[70:71], %[bm] offset:15760\n\t" \
  "v_mfma_f32_16x16x32_bf16 v[228:231], v[72:75], v[192:195], v[228:231]\n\t" \
  "ds_read_b64_tr_b16 v[72:73], %[bm] offset:15648\n\t" \
  "ds_read_b64_tr_b16 v[74:75], %[bm] offset:15792\n\t" \
  "v_mfma_f32_16x16x32_bf16 v[232:235], v[76:79], v[192:195], v[232:235]\n\t" \
  "ds_read_b64_tr_b16 v[76:77], %[bm] offset:15680\n\t" \
  "ds_read_b64_tr_b16 v[78:79], %[bm] offset:15824\n\t" \
  "s_add_u32 %[t], %[so], 442368\n\t" \
  "buffer_load_dwordx4 v[192:195], %[mo], %[ws], %[t] offen sc1\n\t" \
  "v_mfma_f32_16x16x32_bf16 v[216:219], v[80:83], v[196:199], v[216:219]\n\t" \
  "ds_read_b64_tr_b16 v[80:81], %[bm] offset:20736\n\t" \
  "ds_read_b64_tr_b16 v[82:83], %[bm] offset:20880\n\t" \
  "v_mfma_f32_16x16x32_bf16 v[220:223], v[84:87], v[196:199], v[220:223]\n\t" \
  "ds_read_b64_tr_b16 v[84:85], %[bm] offset:20768\n\t" \
  "ds_read_b64_tr_b16 v[86:87], %[bm] offset:20912\n\t" \
  "v_mfma_f32_16x16x32_bf16 v[224:227], v[88:91], v[196:199], v[224:227]\n\t" \
  "ds_read_b64_tr_b16 v[88:89], %[bm] offset:20800\n\t" \
  "ds_read_b64_tr_b16 v[90:91], %[bm] offset:20944\n\t" \
  "v_mfma_f32_16x16x32_bf16 v[228:231], v[92:95], v[196:199], v[228:231]\n\t" \
  "ds_read_b64_tr_b16 v[92:93], %[bm] offset:20832\n\t" \
  "ds_read_b64_tr_b16 v[94:95], %[bm] offset:20976\n\t" \
  "v_mfma_f32_16x16x32_bf16 v[232:235], v[96:99], v[196:199], v[232:235]\n\t" \
  "ds_read_b64_tr_b16 v[96:97], %[bm] offset:20864\n\t" \
  "ds_read_b64_tr_b16 v[98:99], %[bm] offset:21008\n\t" \
  "s_add_u32 %[t], %[so], 589824\n\t" \
  "buffer_load_dwordx4 v[196:199], %[mo], %[ws], %[t] offen sc1\n\t" \
  "v_mfma_f32_16x16x32_bf16 v[216:219], v[100:103], v[200:203], v[216:219]\n\t" \
  "ds_read_b64_tr_b16 v[100:101], %[bm] offset:25920\n\t" \
  "ds_read_b64_tr_b16 v[102:103], %[bm] offset:26064\n\t" \
  "v_mfma_f32_16x16x32_bf16 v[220:223], v[104:107], v[200:203], v[220:223]\n\t" \
  "ds_read_b64_tr_b16 v[104:105], %[bm] offset:25952\n\t" \
  "ds_read_b64_tr_b16 v[106:107], %[bm] offset:26096\n\t" \
  "v_mfma_f32_16x16x32_bf16 v[224:227], v[108:111], v[200:203], v[224:227]\n\t" \
  "ds_read_b64_tr_b16 v[108:109], %[bm] offset:25984\n\t" \
  "ds_read_b64_tr_b16 v[110:111], %[bm] offset:26128\n\t" \
  "v_mfma_f32_16x16x32_bf16 v[228:231], v[112:115], v[200:203], v[228:231]\n\t" \
  "ds_read_b64_tr_b16 v[112:113], %[bm] offset:26016\n\t" \
  "ds_read_b64_tr_b16 v[114:115], %[bm] offset:26160\n\t" \
  "v_mfma_f32_16x16x32_bf16 v[232:235], v[116:119], v[200:203], v[232:235]\n\t" \
  "ds_read_b64_tr_b16 v[116:117], %[bm] offset:26048\n\t" \
  "ds_read_b64_tr_b16 v[118:119], %[bm] offset:26192\n\t" \
  "s_add_u32 %[t], %[so], 737280\n\t" \
  "buffer_load_dwordx4 v[200:203], %[mo], %[ws], %[t] offen sc1\n\t" \
  "v_mfma_f32_16x16x32_bf16 v[216:219], v[120:123], v[204:207], v[216:219]\n\t" \
  "ds_read_b64_tr_b16 v[120:121], %[bm] offset:31104\n\t" \
  "ds_read_b64_tr_b16 v[122:123], %[bm] offset:31248\n\t" \
  "v_mfma_f32_16x16x32_bf16 v[220:223], v[124:127], v[204:207], v[220:223]\n\t" \
  "ds_read_b64_tr_b16 v[124:125], %[bm] offset:31136\n\t" \
  "ds_read_b64_tr_b16 v[126:127], %[bm] offset:31280\n\t" \
  "v_mfma_f32_16x16x32_bf16 v[224:227], v[128:131], v[204:207], v[224:227]\n\t" \
  "ds_read_b64_tr_b16 v[128:129], %[bm] offset:31168\n\t" \
  "ds_read_b64_tr_b16 v[130:131], %[bm] offset:31312\n\t" \
  "v_mfma_f32_16x16x32_bf16 v[228:231], v[132:135], v[204:207], v[228:231]\n\t" \
  "ds_read_b64_tr_b16 v[132:133], %[bm] offset:31200\n\t" \
  "ds_read_b64_tr_b16 v[134:135], %[bm] offset:31344\n\t" \
  "v_mfma_f32_16x16x32_bf16 v[232:235], v[136:139], v[204:207], v[232:235]\n\t" \
  "ds_read_b64_tr_b16 v[136:137], %[bm] offset:31232\n\t" \
  "ds_read_b64_tr_b16 v[138:139], %[bm] offset:31376\n\t" \
  "s_add_u32 %[t], %[so], 884736\n\t" \
  "buffer_load_dwordx4 v[204:207], %[mo], %[ws], %[t] offen sc1\n\t" \
  "v_mfma_f32_16x16x32_bf16 v[216:219], v[140:143], v[208:211], v[216:219]\n\t" \
  "ds_read_b64_tr_b16 v[140:141], %[bm] offset:36288\n\t" \
  "ds_read_b64_tr_b16 v[142:143], %[bm] offset:36432\n\t" \
  "v_mfma_f32_16x16x32_bf16 v[220:223], v[144:147], v[208:211], v[220:223]\n\t" \
  "ds_read_b64_tr_b16 v[144:145], %[bm] offset:36320\n\t" \
  "ds_read_b64_tr_b16 v[146:147], %[bm] offset:36464\n\t" \
  "v_mfma_f32_16x16x32_bf16 v[224:227], v[148:151], v[208:211], v[224:227]\n\t" \
  "ds_read_b64_tr_b16 v[148:149], %[bm] offset:36352\n\t" \
  "ds_read_b64_tr_b16 v[150:151], %[bm] offset:36496\n\t" \
  "v_mfma_f32_16x16x32_bf16 v[228:231], v[152:155], v[208:211], v[228:231]\n\t" \
  "ds_read_b64_tr_b16 v[152:153], %[bm] offset:36384\n\t" \
  "ds_read_b64_tr_b16 v[154:155], %[bm] offset:36528\n\t" \
  "v_mfma_f32_16x16x32_bf16 v[232:235], v[156:159], v[208:211], v[232:235]\n\t" \
  "ds_read_b64_tr_b16 v[156:157], %[bm] offset:36416\n\t" \
  "ds_read_b64_tr_b16 v[158:159], %[bm] offset:36560\n\t" \
  "s_add_u32 %[t], %[so], 1032192\n\t" \
  "buffer_load_dwordx4 v[208:211], %[mo], %[ws], %[t] offen sc1\n\t" \
  "v_mfma_f32_16x16x32_bf16 v[216:219], v[160:163], v[212:215], v[216:219]\n\t" \
  "ds_read_b64_tr_b16 v[160:161], %[br] offset:0\n\t" \
  "ds_read_b64_tr_b16 v[162:163], %[br] offset:5184\n\t" \
  "v_mfma_f32_16x16x32_bf16 v[220:223], v[164:167], v[212:215], v[220:223]\n\t" \
  "ds_read_b64_tr_b16 v[164:165], %[br] offset:32\n\t" \
  "ds_read_b64_tr_b16 v[166:167], %[br] offset:5216\n\t" \
  "v_mfma_f32_16x16x32_bf16 v[224:227], v[168:171], v[212:215], v[224:227]\n\t" \
  "ds_read_b64_tr_b16 v[168:169], %[br] offset:64\n\t" \
  "ds_read_b64_tr_b16 v[170:171], %[br] offset:5248\n\t" \
  "v_mfma_f32_16x16x32_bf16 v[228:231], v[172:175], v[212:215], v[228:231]\n\t" \
  "ds_read_b64_tr_b16 v[172:173], %[br] offset:96\n\t" \
  "ds_read_b64_tr_b16 v[174:175], %[br] offset:5280\n\t" \
  "v_mfma_f32_16x16x32_bf16 v[232:235], v[176:179], v[212:215], v[232:235]\n\t" \
  "ds_read_b64_tr_b16 v[176:177], %[br] offset:128\n\t" \
  "ds_read_b64_tr_b16 v[178:179], %[br] offset:5312\n\t" \
  "s_add_u32 %[t], %[so], 0\n\t" \
  "buffer_load_dword v212, %[ro], %[ws], %[t] offen sc1\n\t" \
  "s_add_u32 %[t], %[so], 73728\n\t" \
  "buffer_load_dword v213, %[ro], %[ws], %[t] offen sc1\n\t" \
  "s_add_u32 %[t], %[so], 147456\n\t" \
  "buffer_load_dword v214, %[ro], %[ws], %[t] offen sc1\n\t" \
  "s_add_u32 %[t], %[so], 221184\n\t" \
  "buffer_load_dword v215, %[ro], %[ws], %[t] offen sc1\n\t"
#define P3_RDA \
  "s_add_u32 %[t], %[so], 0\n\t" \
  "buffer_load_dwordx4 v[180:183], %[mo], %[ws], %[t] offen sc1\n\t" \
  "s_add_u32 %[t], %[so], 147456\n\t" \
  "buffer_load_dwordx4 v[184:187], %[mo], %[ws], %[t] offen sc1\n\t" \
  "s_add_u32 %[t], %[so], 294912\n\t" \
  "buffer_load_dwordx4 v[188:191], %[mo], %[ws], %[t] offen sc1\n\t" \
  "s_add_u32 %[t], %[so], 442368\n\t" \
  "buffer_load_dwordx4 v[192:195], %[mo], %[ws], %[t] offen sc1\n\t" \
  "s_add_u32 %[t], %[so], 589824\n\t" \
  "buffer_load_dwordx4 v[196:199], %[mo], %[ws], %[t] offen sc1\n\t" \
  "s_add_u32 %[t], %[so], 737280\n\t" \
  "buffer_load_dwordx4 v[200:203], %[mo], %[ws], %[t] offen sc1\n\t" \
  "s_add_u32 %[t], %[so], 884736\n\t" \
  "buffer_load_dwordx4 v[204:207], %[mo], %[ws], %[t] offen sc1\n\t" \
  "s_add_u32 %[t], %[so], 1032192\n\t" \
  "buffer_load_dwordx4 v[208:211], %[mo], %[ws], %[t] offen sc1\n\t" \
  "s_add_u32 %[t], %[so], 0\n\t" \
  "buffer_load_dword v212, %[ro], %[ws], %[t] offen sc1\n\t" \
  "s_add_u32 %[t], %[so], 73728\n\t" \
  "buffer_load_dword v213, %[ro], %[ws], %[t] offen sc1\n\t" \
  "s_add_u32 %[t], %[so], 147456\n\t" \
  "buffer_load_dword v214, %[ro], %[ws], %[t] offen sc1\n\t" \
  "s_add_u32 %[t], %[so], 221184\n\t" \
  "buffer_load_dword v215, %[ro], %[ws], %[t] offen sc1\n\t" \
  "ds_read_b64_tr_b16 v[0:1], %[bm] offset:0\n\t" \
  "ds_read_b64_tr_b16 v[2:3], %[bm] offset:144\n\t" \
  "ds_read_b64_tr_b16 v[4:5], %[bm] offset:32\n\t" \
  "ds_read_b64_tr_b16 v[6:7], %[bm] offset:176\n\t" \
  "ds_read_b64_tr_b16 v[8:9], %[bm] offset:64\n\t" \
  "ds_read_b64_tr_b16 v[10:11], %[bm] offset:208\n\t" \
  "ds_read_b64_tr_b16 v[12:13], %[bm] offset:96\n\t" \
  "ds_read_b64_tr_b16 v[14:15], %[bm] offset:240\n\t" \
  "ds_read_b64_tr_b16 v[16:17], %[bm] offset:128\n\t" \
  "ds_read_b64_tr_b16 v[18:19], %[bm] offset:272\n\t" \
  "ds_read_b64_tr_b16 v[20:21], %[bm] offset:5184\n\t" \
  "ds_read_b64_tr_b16 v[22:23], %[bm] offset:5328\n\t" \
  "ds_read_b64_tr_b16 v[24:25], %[bm] offset:5216\n\t" \
  "ds_read_b64_tr_b16 v[26:27], %[bm] offset:5360\n\t" \
  "ds_read_b64_tr_b16 v[28:29], %[bm] offset:5248\n\t" \
  "ds_read_b64_tr_b16 v[30:31], %[bm] offset:5392\n\t" \
  "ds_read_b64_tr_b16 v[32:33], %[bm] offset:5280\n\t" \
  "ds_read_b64_tr_b16 v[34:35], %[bm] offset:5424\n\t" \
  "ds_read_b64_tr_b16 v[36:37], %[bm] offset:5312\n\t" \
  "ds_read_b64_tr_b16 v[38:39], %[bm] offset:5456\n\t" \
  "ds_read_b64_tr_b16 v[40:41], %[bm] offset:10368\n\t" \
  "ds_read_b64_tr_b16 v[42:43], %[bm] offset:10512\n\t" \
  "ds_read_b64_tr_b16 v[44:45], %[bm] offset:10400\n\t" \
  "ds_read_b64_tr_b16 v[46:47], %[bm] offset:10544\n\t" \
  "ds_read_b64_tr_b16 v[48:49], %[bm] offset:10432\n\t" \
  "ds_read_b64_tr_b16 v[50:51], %[bm] offset:10576\n\t" \
  "ds_read_b64_tr_b16 v[52:53], %[bm] offset:10464\n\t" \
  "ds_read_b64_tr_b16 v[54:55], %[bm] offset:10608\n\t" \
  "ds_read_b64_tr_b16 v[56:57], %[bm] offset:10496\n\t" \
  "ds_read_b64_tr_b16 v[58:59], %[bm] offset:10640\n\t" \
  "ds_read_b64_tr_b16 v[60:61], %[bm] offset:15552\n\t" \
  "ds_read_b64_tr_b16 v[62:63], %[bm] offset:15696\n\t" \
  "ds_read_b64_tr_b16 v[64:65], %[bm] offset:15584\n\t" \
  "ds_read_b64_tr_b16 v[66:67], %[bm] offset:15728\n\t" \
  "ds_read_b64_tr_b16 v[68:69], %[bm] offset:15616\n\t" \
  "ds_read_b64_tr_b16 v[70:71], %[bm] offset:15760\n\t" \
  "ds_read_b64_tr_b16 v[72:73], %[bm] offset:15648\n\t" \
  "ds_read_b64_tr_b16 v[74:75], %[bm] offset:15792\n\t" \
  "ds_read_b64_tr_b16 v[76:77], %[bm] offset:15680\n\t" \
  "ds_read_b64_tr_b16 v[78:79], %[bm] offset:15824\n\t" \
  "ds_read_b64_tr_b16 v[80:81], %[bm] offset:20736\n\t" \
  "ds_read_b64_tr_b16 v[82:83], %[bm] offset:20880\n\t" \
  "ds_read_b64_tr_b16 v[84:85], %[bm] offset:20768\n\t" \
  "ds_read_b64_tr_b16 v[86:87], %[bm] offset:20912\n\t" \
  "ds_read_b64_tr_b16 v[88:89], %[bm] offset:20800\n\t" \
  "ds_read_b64_tr_b16 v[90:91], %[bm] offset:20944\n\t" \
  "ds_read_b64_tr_b16 v[92:93], %[bm] offset:20832\n\t" \
  "ds_read_b64_tr_b16 v[94:95], %[bm] offset:20976\n\t" \
  "ds_read_b64_tr_b16 v[96:97], %[bm] offset:20864\n\t" \
  "ds_read_b64_tr_b16 v[98:99], %[bm] offset:21008\n\t" \
  "ds_read_b64_tr_b16 v[100:101], %[bm] offset:25920\n\t" \
  "ds_read_b64_tr_b16 v[102:103], %[bm] offset:26064\n\t" \
  "ds_read_b64_tr_b16 v[104:105], %[bm] offset:25952\n\t" \
  "ds_read_b64_tr_b16 v[106:107], %[bm] offset:26096\n\t" \
  "ds_read_b64_tr_b16 v[108:109], %[bm] offset:25984\n\t" \
  "ds_read_b64_tr_b16 v[110:111], %[bm] offset:26128\n\t" \
  "ds_read_b64_tr_b16 v[112:113], %[bm] offset:26016\n\t" \
  "ds_read_b64_tr_b16 v[114:115], %[bm] offset:26160\n\t" \
  "ds_read_b64_tr_b16 v[116:117], %[bm] offset:26048\n\t" \
  "ds_read_b64_tr_b16 v[118:119], %[bm] offset:26192\n\t" \
  "ds_read_b64_tr_b16 v[120:121], %[bm] offset:31104\n\t" \
  "ds_read_b64_tr_b16 v[122:123], %[bm] offset:31248\n\t" \
  "ds_read_b64_tr_b16 v[124:125], %[bm] offset:31136\n\t" \
  "ds_read_b64_tr_b16 v[126:127], %[bm] offset:31280\n\t" \
  "ds_read_b64_tr_b16 v[128:129], %[bm] offset:31168\n\t" \
  "ds_read_b64_tr_b16 v[130:131], %[bm] offset:31312\n\t" \
  "ds_read_b64_tr_b16 v[132:133], %[bm] offset:31200\n\t" \
  "ds_read_b64_tr_b16 v[134:135], %[bm] offset:31344\n\t" \
  "ds_read_b64_tr_b16 v[136:137], %[bm] offset:31232\n\t" \
  "ds_read_b64_tr_b16 v[138:139], %[bm] offset:31376\n\t" \
  "ds_read_b64_tr_b16 v[140:141], %[bm] offset:36288\n\t" \
  "ds_read_b64_tr_b16 v[142:143], %[bm] offset:36432\n\t" \
  "ds_read_b64_tr_b16 v[144:145], %[bm] offset:36320\n\t" \
  "ds_read_b64_tr_b16 v[146:147], %[bm] offset:36464\n\t" \
  "ds_read_b64_tr_b16 v[148:149], %[bm] offset:36352\n\t" \
  "ds_read_b64_tr_b16 v[150:151], %[bm] offset:36496\n\t" \
  "ds_read_b64_tr_b16 v[152:153], %[bm] offset:36384\n\t" \
  "ds_read_b64_tr_b16 v[154:155], %[bm] offset:36528\n\t" \
  "ds_read_b64_tr_b16 v[156:157], %[bm] offset:36416\n\t" \
  "ds_read_b64_tr_b16 v[158:159], %[bm] offset:36560\n\t" \
  "ds_read_b64_tr_b16 v[160:161], %[br] offset:0\n\t" \
  "ds_read_b64_tr_b16 v[162:163], %[br] offset:5184\n\t" \
  "ds_read_b64_tr_b16 v[164:165], %[br] offset:32\n\t" \
  "ds_read_b64_tr_b16 v[166:167], %[br] offset:5216\n\t" \
  "ds_read_b64_tr_b16 v[168:169], %[br] offset:64\n\t" \
  "ds_read_b64_tr_b16 v[170:171], %[br] offset:5248\n\t" \
  "ds_read_b64_tr_b16 v[172:173], %[br] offset:96\n\t" \
  "ds_read_b64_tr_b16 v[174:175], %[br] offset:5280\n\t" \
  "ds_read_b64_tr_b16 v[176:177], %[br] offset:128\n\t" \
  "ds_read_b64_tr_b16 v[178:179], %[br] offset:5312\n\t" \
  "s_mov_b32 %[sp], 0x400000\n\t" \
  ".Lra_%=:\n\t" \
  "ds_read_b32 %[x], %[su] offset:24\n\t" \
  "ds_read_b128 v[216:219], %[xa] offset:0\n\t" \
  "ds_read_b128 v[220:223], %[xa] offset:1024\n\t" \
  "ds_read_b128 v[224:227], %[xa] offset:2048\n\t" \
  "ds_read_b128 v[228:231], %[xa] offset:3072\n\t" \
  "ds_read_b128 v[232:235], %[xa] offset:4096\n\t" \
  "s_waitcnt lgkmcnt(0)\n\t" \
  "v_readfirstlane_b32 %[t], %[x]\n\t" \
  "s_sub_u32 %[sp], %[sp], 1\n\t" \
  "s_cbranch_scc1 .Lrb_%=\n\t" \
  "s_cmp_lt_u32 %[t], 4\n\t" \
  "s_cbranch_scc1 .Lra_%=\n\t" \
  ".Lrb_%=:\n\t"
#define P3_FIN \
  "s_mov_b32 %[sp], 0x40000\n\t" \
  ".Lfw0_%=:\n\t" \
  "s_waitcnt vmcnt(11) lgkmcnt(15)\n\t" \
  "v_max3_u32 %[x], v180, v181, v182\n\t" \
  "v_max_u32 %[x], v183, %[x]\n\t" \
  "v_cmp_eq_u32 vcc, -1, %[x]\n\t" \
  "s_cbranch_vccnz .Lfr0_%=\n\t" \
  ".Lfc0_%=:\n\t" \
  "v_mfma_f32_16x16x32_bf16 v[216:219], v[0:3], v[180:183], v[216:219]\n\t" \
  "v_mfma_f32_16x16x32_bf16 v[220:223], v[4:7], v[180:183], v[220:223]\n\t" \
  "v_mfma_f32_16x16x32_bf16 v[224:227], v[8:11], v[180:183], v[224:227]\n\t" \
  "v_mfma_f32_16x16x32_bf16 v[228:231], v[12:15], v[180:183], v[228:231]\n\t" \
  "v_mfma_f32_16x16x32_bf16 v[232:235], v[16:19], v[180:183], v[232:235]\n\t" \
  ".Lfw1_%=:\n\t" \
  "s_waitcnt vmcnt(10) lgkmcnt(15)\n\t" \
  "v_max3_u32 %[x], v184, v185, v186\n\t" \
  "v_max_u32 %[x], v187, %[x]\n\t" \
  "v_cmp_eq_u32 vcc, -1, %[x]\n\t" \
  "s_cbranch_vccnz .Lfr1_%=\n\t" \
  ".Lfc1_%=:\n\t" \
  "v_mfma_f32_16x16x32_bf16 v[216:219], v[20:23], v[184:187], v[216:219]\n\t" \
  "v_mfma_f32_16x16x32_bf16 v[220:223], v[24:27], v[184:187], v[220:223]\n\t" \
  "v_mfma_f32_16x16x32_bf16 v[224:227], v[28:31], v[184:187], v[224:227]\n\t" \
  "v_mfma_f32_16x16x32_bf16 v[228:231], v[32:35], v[184:187], v[228:231]\n\t" \
  "v_mfma_f32_16x16x32_bf16 v[232:235], v[36:39], v[184:187], v[232:235]\n\t" \
  ".Lfw2_%=:\n\t" \
  "s_waitcnt vmcnt(9) lgkmcnt(15)\n\t" \
  "v_max3_u32 %[x], v188, v189, v190\n\t" \
  "v_max_u32 %[x], v191, %[x]\n\t" \
  "v_cmp_eq_u32 vcc, -1, %[x]\n\t" \
  "s_cbranch_vccnz .Lfr2_%=\n\t" \
  ".Lfc2_%=:\n\t" \
  "v_mfma_f32_16x16x32_bf16 v[216:219], v[40:43], v[188:191], v[216:219]\n\t" \
  "v_mfma_f32_16x16x32_bf16 v[220:223], v[44:47], v[188:191], v[220:223]\n\t" \
  "v_mfma_f32_16x16x32_bf16 v[224:227], v[48:51], v[188:191], v[224:227]\n\t" \
  "v_mfma_f32_16x16x32_bf16 v[228:231], v[52:55], v[188:191], v[228:231]\n\t" \
  "v_mfma_f32_16x16x32_bf16 v[232:235], v[56:59], v[188:191], v[232:235]\n\t" \
  ".Lfw3_%=:\n\t" \
  "s_waitcnt vmcnt(8) lgkmcnt(15)\n\t" \
  "v_max3_u32 %[x], v192, v193, v194\n\t" \
  "v_max_u32 %[x], v195, %[x]\n\t" \
  "v_cmp_eq_u32 vcc, -1, %[x]\n\t" \
  "s_cbranch_vccnz .Lfr3_%=\n\t" \
  ".Lfc3_%=:\n\t" \
  "v_mfma_f32_16x16x32_bf16 v[216:219], v[60:63], v[192:195], v[216:219]\n\t" \
  "v_mfma_f32_16x16x32_bf16 v[220:223], v[64:67], v[192:195], v[220:223]\n\t" \
  "v_mfma_f32_16x16x32_bf16 v[224:227], v[68:71], v[192:195], v[224:227]\n\t" \
  "v_mfma_f32_16x16x32_bf16 v[228:231], v[72:75], v[192:195], v[228:231]\n\t" \
  "v_mfma_f32_16x16x32_bf16 v[232:235], v[76:79], v[192:195], v[232:235]\n\t" \
  ".Lfw4_%=:\n\t" \
  "s_waitcnt vmcnt(7) lgkmcnt(15)\n\t" \
  "v_max3_u32 %[x], v196, v197, v198\n\t" \
  "v_max_u32 %[x], v199, %[x]\n\t" \
  "v_cmp_eq_u32 vcc, -1, %[x]\n\t" \
  "s_cbranch_vccnz .Lfr4_%=\n\t" \
  ".Lfc4_%=:\n\t" \
  "v_mfma_f32_16x16x32_bf16 v[216:219], v[80:83], v[196:199], v[216:219]\n\t" \
  "v_mfma_f32_16x16x32_bf16 v[220:223], v[84:87], v[196:199], v[220:223]\n\t" \
  "v_mfma_f32_16x16x32_bf16 v[224:227], v[88:91], v[196:199], v[224:227]\n\t" \
  "v_mfma_f32_16x16x32_bf16 v[228:231], v[92:95], v[196:199], v[228:231]\n\t" \
  "v_mfma_f32_16x16x32_bf16 v[232:235], v[96:99], v[196:199], v[232:235]\n\t" \
  ".Lfw5_%=:\n\t" \
  "s_waitcnt vmcnt(6) lgkmcnt(15)\n\t" \
  "v_max3_u32 %[x], v200, v201, v202\n\t" \
  "v_max_u32 %[x], v203, %[x]\n\t" \
  "v_cmp_eq_u32 vcc, -1, %[x]\n\t" \
  "s_cbranch_vccnz .Lfr5_%=\n\t" \
  ".Lfc5_%=:\n\t" \
  "v_mfma_f32_16x16x32_bf16 v[216:219], v[100:103], v[200:203], v[216:219]\n\t" \
  "v_mfma_f32_16x16x32_bf16 v[220:223], v[104:107], v[200:203], v[220:223]\n\t" \
  "v_mfma_f32_16x16x32_bf16 v[224:227], v[108:111], v[200:203], v[224:227]\n\t" \
  "v_mfma_f32_16x16x32_bf16 v[228:231], v[112:115], v[200:203], v[228:231]\n\t" \
  "v_mfma_f32_16x16x32_bf16 v[232:235], v[116:119], v[200:203], v[232:235]\n\t" \
  ".Lfw6_%=:\n\t" \
  "s_waitcnt vmcnt(5) lgkmcnt(15)\n\t" \
  "v_max3_u32 %[x], v204, v205, v206\n\t" \
  "v_max_u32 %[x], v207, %[x]\n\t" \
  "v_cmp_eq_u32 vcc, -1, %[x]\n\t" \
  "s_cbranch_vccnz .Lfr6_%=\n\t" \
  ".Lfc6_%=:\n\t" \
  "v_mfma_f32_16x16x32_bf16 v[216:219], v[120:123], v[204:207], v[216:219]\n\t" \
  "v_mfma_f32_16x16x32_bf16 v[220:223], v[124:127], v[204:207], v[220:223]\n\t" \
  "v_mfma_f32_16x16x32_bf16 v[224:227], v[128:131], v[204:207], v[224:227]\n\t" \
  "v_mfma_f32_16x16x32_bf16 v[228:231], v[132:135], v[204:207], v[228:231]\n\t" \
  "v_mfma_f32_16x16x32_bf16 v[232:235], v[136:139], v[204:207], v[232:235]\n\t" \
  ".Lfw7_%=:\n\t" \
  "s_waitcnt vmcnt(4) lgkmcnt(10)\n\t" \
  "v_max3_u32 %[x], v208, v209, v210\n\t" \
  "v_max_u32 %[x], v211, %[x]\n\t" \
  "v_cmp_eq_u32 vcc, -1, %[x]\n\t" \
  "s_cbranch_vccnz .Lfr7_%=\n\t" \
  ".Lfc7_%=:\n\t" \
  "v_mfma_f32_16x16x32_bf16 v[216:219], v[140:143], v[208:211], v[216:219]\n\t" \
  "v_mfma_f32_16x16x32_bf16 v[220:223], v[144:147], v[208:211], v[220:223]\n\t" \
  "v_mfma_f32_16x16x32_bf16 v[224:227], v[148:151], v[208:211], v[224:227]\n\t" \
  "v_mfma_f32_16x16x32_bf16 v[228:231], v[152:155], v[208:211], v[228:231]\n\t" \
  "v_mfma_f32_16x16x32_bf16 v[232:235], v[156:159], v[208:211], v[232:235]\n\t" \
  ".Lfw8_%=:\n\t" \
  "s_waitcnt vmcnt(0) lgkmcnt(0)\n\t" \
  "v_max3_u32 %[x], v212, v213, v214\n\t" \
  "v_max_u32 %[x], v215, %[x]\n\t" \
  "v_cmp_eq_u32 vcc, -1, %[x]\n\t" \
  "s_cbranch_vccnz .Lfr8_%=\n\t" \
  ".Lfc8_%=:\n\t" \
  "v_mfma_f32_16x16x32_bf16 v[216:219], v[160:163], v[212:215], v[216:219]\n\t" \
  "v_mfma_f32_16x16x32_bf16 v[220:223], v[164:167], v[212:215], v[220:223]\n\t" \
  "v_mfma_f32_16x16x32_bf16 v[224:227], v[168:171], v[212:215], v[224:227]\n\t" \
  "v_mfma_f32_16x16x32_bf16 v[228:231], v[172:175], v[212:215], v[228:231]\n\t" \
  "v_mfma_f32_16x16x32_bf16 v[232:235], v[176:179], v[212:215], v[232:235]\n\t" \
  "s_branch .Lfe_%=\n\t" \
  ".Lfr0_%=:\n\t" \
  "s_sub_u32 %[sp], %[sp], 1\n\t" \
  "s_cmp_lt_i32 %[sp], 0\n\t" \
  "s_cbranch_scc1 .Lfc0_%=\n\t" \
  "s_waitcnt vmcnt(0)\n\t" \
  "s_sleep 3\n\t" \
  "s_call_b64 %[ret], .Lfl0_%=\n\t" \
  "s_branch .Lfw0_%=\n\t" \
  ".Lfr1_%=:\n\t" \
  "s_sub_u32 %[sp], %[sp], 1\n\t" \
  "s_cmp_lt_i32 %[sp], 0\n\t" \
  "s_cbranch_scc1 .Lfc1_%=\n\t" \
  "s_waitcnt vmcnt(0)\n\t" \
  "s_sleep 3\n\t" \
  "s_call_b64 %[ret], .Lfl1_%=\n\t" \
  "s_branch .Lfw1_%=\n\t" \
  ".Lfr2_%=:\n\t" \
  "s_sub_u32 %[sp], %[sp], 1\n\t" \
  "s_cmp_lt_i32 %[sp], 0\n\t" \
  "s_cbranch_scc1 .Lfc2_%=\n\t" \
  "s_waitcnt vmcnt(0)\n\t" \
  "s_sleep 3\n\t" \
  "s_call_b64 %[ret], .Lfl2_%=\n\t" \
  "s_branch .Lfw2_%=\n\t" \
  ".Lfr3_%=:\n\t" \
  "s_sub_u32 %[sp], %[sp], 1\n\t" \
  "s_cmp_lt_i32 %[sp], 0\n\t" \
  "s_cbranch_scc1 .Lfc3_%=\n\t" \
  "s_waitcnt vmcnt(0)\n\t" \
  "s_sleep 3\n\t" \
  "s_call_b64 %[ret], .Lfl3_%=\n\t" \
  "s_branch .Lfw3_%=\n\t" \
  ".Lfr4_%=:\n\t" \
  "s_sub_u32 %[sp], %[sp], 1\n\t" \
  "s_cmp_lt_i32 %[sp], 0\n\t" \
  "s_cbranch_scc1 .Lfc4_%=\n\t" \
  "s_waitcnt vmcnt(0)\n\t" \
  "s_sleep 3\n\t" \
  "s_call_b64 %[ret], .Lfl4_%=\n\t" \
  "s_branch .Lfw4_%=\n\t" \
  ".Lfr5_%=:\n\t" \
  "s_sub_u32 %[sp], %[sp], 1\n\t" \
  "s_cmp_lt_i32 %[sp], 0\n\t" \
  "s_cbranch_scc1 .Lfc5_%=\n\t" \
  "s_waitcnt vmcnt(0)\n\t" \
  "s_sleep 3\n\t" \
  "s_call_b64 %[ret], .Lfl5_%=\n\t" \
  "s_branch .Lfw5_%=\n\t" \
  ".Lfr6_%=:\n\t" \
  "s_sub_u32 %[sp], %[sp], 1\n\t" \
  "s_cmp_lt_i32 %[sp], 0\n\t" \
  "s_cbranch_scc1 .Lfc6_%=\n\t" \
  "s_waitcnt vmcnt(0)\n\t" \
  "s_sleep 3\n\t" \
  "s_call_b64 %[ret], .Lfl6_%=\n\t" \
  "s_branch .Lfw6_%=\n\t" \
  ".Lfr7_%=:\n\t" \
  "s_sub_u32 %[sp], %[sp], 1\n\t" \
  "s_cmp_lt_i32 %[sp], 0\n\t" \
  "s_cbranch_scc1 .Lfc7_%=\n\t" \
  "s_waitcnt vmcnt(0)\n\t" \
  "s_sleep 3\n\t" \
  "s_call_b64 %[ret], .Lfl7_%=\n\t" \
  "s_branch .Lfw7_%=\n\t" \
  ".Lfr8_%=:\n\t" \
  "s_sub_u32 %[sp], %[sp], 1\n\t" \
  "s_cmp_lt_i32 %[sp], 0\n\t" \
  "s_cbranch_scc1 .Lfc8_%=\n\t" \
  "s_waitcnt vmcnt(0)\n\t" \
  "s_sleep 3\n\t" \
  "s_call_b64 %[ret], .Lfl8_%=\n\t" \
  "s_branch .Lfw8_%=\n\t" \
  ".Lfl0_%=:\n\t" \
  "s_add_u32 %[t], %[so], 0\n\t" \
  "buffer_load_dwordx4 v[180:183], %[mo], %[ws], %[t] offen sc1\n\t" \
  ".Lfs0_%=:\n\t" \
  "v_max3_u32 %[x], v184, v185, v186\n\t" \
  "v_max_u32 %[x], v187, %[x]\n\t" \
  "v_cmp_eq_u32 vcc, -1, %[x]\n\t" \
  "s_cbranch_vccz .Lfs1_%=\n\t" \
  ".Lfl1_%=:\n\t" \
  "s_add_u32 %[t], %[so], 147456\n\t" \
  "buffer_load_dwordx4 v[184:187], %[mo], %[ws], %[t] offen sc1\n\t" \
  ".Lfs1_%=:\n\t" \
  "v_max3_u32 %[x], v188, v189, v190\n\t" \
  "v_max_u32 %[x], v191, %[x]\n\t" \
  "v_cmp_eq_u32 vcc, -1, %[x]\n\t" \
  "s_cbranch_vccz .Lfs2_%=\n\t" \
  ".Lfl2_%=:\n\t" \
  "s_add_u32 %[t], %[so], 294912\n\t" \
  "buffer_load_dwordx4 v[188:191], %[mo], %[ws], %[t] offen sc1\n\t" \
  ".Lfs2_%=:\n\t" \
  "v_max3_u32 %[x], v192, v193, v194\n\t" \
  "v_max_u32 %[x], v195, %[x]\n\t" \
  "v_cmp_eq_u32 vcc, -1, %[x]\n\t" \
  "s_cbranch_vccz .Lfs3_%=\n\t" \
  ".Lfl3_%=:\n\t" \
  "s_add_u32 %[t], %[so], 442368\n\t" \
  "buffer_load_dwordx4 v[192:195], %[mo], %[ws], %[t] offen sc1\n\t" \
  ".Lfs3_%=:\n\t" \
  "v_max3_u32 %[x], v196, v197, v198\n\t" \
  "v_max_u32 %[x], v199, %[x]\n\t" \
  "v_cmp_eq_u32 vcc, -1, %[x]\n\t" \
  "s_cbranch_vccz .Lfs4_%=\n\t" \
  ".Lfl4_%=:\n\t" \
  "s_add_u32 %[t], %[so], 589824\n\t" \
  "buffer_load_dwordx4 v[196:199], %[mo], %[ws], %[t] offen sc1\n\t" \
  ".Lfs4_%=:\n\t" \
  "v_max3_u32 %[x], v200, v201, v202\n\t" \
  "v_max_u32 %[x], v203, %[x]\n\t" \
  "v_cmp_eq_u32 vcc, -1, %[x]\n\t" \
  "s_cbranch_vccz .Lfs5_%=\n\t" \
  ".Lfl5_%=:\n\t" \
  "s_add_u32 %[t], %[so], 737280\n\t" \
  "buffer_load_dwordx4 v[200:203], %[mo], %[ws], %[t] offen sc1\n\t" \
  ".Lfs5_%=:\n\t" \
  "v_max3_u32 %[x], v204, v205, v206\n\t" \
  "v_max_u32 %[x], v207, %[x]\n\t" \
  "v_cmp_eq_u32 vcc, -1, %[x]\n\t" \
  "s_cbranch_vccz .Lfs6_%=\n\t" \
  ".Lfl6_%=:\n\t" \
  "s_add_u32 %[t], %[so], 884736\n\t" \
  "buffer_load_dwordx4 v[204:207], %[mo], %[ws], %[t] offen sc1\n\t" \
  ".Lfs6_%=:\n\t" \
  "v_max3_u32 %[x], v208, v209, v210\n\t" \
  "v_max_u32 %[x], v211, %[x]\n\t" \
  "v_cmp_eq_u32 vcc, -1, %[x]\n\t" \
  "s_cbranch_vccz .Lfs7_%=\n\t" \
  ".Lfl7_%=:\n\t" \
  "s_add_u32 %[t], %[so], 1032192\n\t" \
  "buffer_load_dwordx4 v[208:211], %[mo], %[ws], %[t] offen sc1\n\t" \
  ".Lfs7_%=:\n\t" \
  "v_max3_u32 %[x], v212, v213, v214\n\t" \
  "v_max_u32 %[x], v215, %[x]\n\t" \
  "v_cmp_eq_u32 vcc, -1, %[x]\n\t" \
  "s_cbranch_vccz .Lfs8_%=\n\t" \
  ".Lfl8_%=:\n\t" \
  "s_add_u32 %[t], %[so], 0\n\t" \
  "buffer_load_dword v212, %[ro], %[ws], %[t] offen sc1\n\t" \
  "s_add_u32 %[t], %[so], 73728\n\t" \
  "buffer_load_dword v213, %[ro], %[ws], %[t] offen sc1\n\t" \
  "s_add_u32 %[t], %[so], 147456\n\t" \
  "buffer_load_dword v214, %[ro], %[ws], %[t] offen sc1\n\t" \
  "s_add_u32 %[t], %[so], 221184\n\t" \
  "buffer_load_dword v215, %[ro], %[ws], %[t] offen sc1\n\t" \
  ".Lfs8_%=:\n\t" \
  "s_waitcnt vmcnt(0)\n\t" \
  "s_setpc_b64 %[ret]\n\t" \
  ".Lfe_%=:\n\t" \
  ".Lgm_last:\n\t" \
  "s_nop 6\n\t"

typedef __attribute__((address_space(1))) unsigned long long gu64;
constexpr int F_EP = WS_FLG, F_ALL = WS_FLG + 2048, F_A = WS_FLG + 4096, F_OB = F_A + 256 * 384, F_B = F_OB + 256 * 256;
static_assert(F_B + 256 * 256 <= WS_END, "flag area");
DI unsigned long long ld_flag(const Params& P, int off) { return __hip_atomic_load((gu64*)(P.ws + off), __ATOMIC_RELAXED, __HIP_MEMORY_SCOPE_AGENT); }
template <int SL = 1> DI void poll(const Params& P, int off, unsigned long long tag) {
#pragma nounroll
  for (int spin = 0; spin < (1 << 20); ++spin) { if (__all(ld_flag(P, off) == tag)) break; __builtin_amdgcn_s_sleep(SL); }
  asm volatile("" ::: "memory");
}
DI int fresh(int v) { asm volatile("" : "+v"(v)); return v; }
DI int sfresh(int v) { asm volatile("" : "+s"(v)); return v; }
#define SB0() __builtin_amdgcn_sched_barrier(0)

constexpr int LDS_W = 49152, LDS_SC = LDS_W, LDS_SC9 = LDS_W + 73728 + 18432;
constexpr int LDS_Q = 147 * 1024, LDS_T0 = LDS_Q + 2 * Q_GRP, LDS_SYNC = LDS_T0 + 1024;
constexpr int LDS_EP = LDS_SYNC + 256, LDS_DUMMY = LDS_SYNC + 512, LDS_GW = LDS_SYNC + 1024;
constexpr int LDS_T1 = LDS_GW, LDS_T2 = LDS_GW + 1024;

template <bool WIDE> DI int kmap(int r) {
  const int st = r >> 5, rd = (r >> 4) & 1;
  const int g = WIDE ? (r >> 2) & 3 : 2 * (r & 1) + ((r >> 3) & 1), q = WIDE ? r & 3 : (r >> 1) & 3;
  return 32 * st + 8 * g + 4 * rd + q;
}
template <bool WIDE> DI unsigned woff(int it, int w, int lane, int colbase, int split) {
  constexpr int CH = WIDE ? 18 : 9;
  const int pc = WIDE ? (it < 2 ? 2 * w + it : 16 + (w >> 2)) : (it == 0 ? w : 8);
  const int u = pc * 64 + lane, rr = u / CH, q = u % CH;
  return kmap<WIDE>(rr) * WROW + colbase + q * 16 + (q >= 9 ? split : 0);
}
DI unsigned long long get_tag(ldsp lds) {
  const u32x2 ep = *(LAS u32x2*)(lds + LDS_EP);
  const unsigned long long e = ((unsigned long long)__builtin_amdgcn_readfirstlane(ep[1]) << 32) | (unsigned)__builtin_amdgcn_readfirstlane(ep[0]);
  return e * 0x9E3779B97F4A7C15ull + 0x632BE59BD9B4E019ull;
}
DI void blk2(int& h, int& c) { const int b = blockIdx.x; h = (b & 7) * 4 + ((b >> 3) & 3); c = b >> 5; }

typedef __attribute__((ext_vector_type(32))) unsigned u32x32;
typedef __attribute__((ext_vector_type(3))) unsigned u32x3;
struct P1State { u32x32 x0, x1, x2; u32x16 x3; f32x16 an; f32x4 anr, anr2; u32x8 x5; f32x32 acc; f32x4 accr; u32x32 w0; u32x8 w1; u32x16 a0; u32x3 a1; unsigned gt; };
#define P1_X(c, s) c "{v[0:31]}"(s.x0), c "{v[32:63]}"(s.x1), c "{v[64:95]}"(s.x2), c "{v[96:111]}"(s.x3)
#define P1_ACC(c, s) c "{v[144:175]}"(s.acc), c "{v[176:179]}"(s.accr)
#define P1_ACN(c, s) c "{v[112:127]}"(s.an), c "{v[128:131]}"(s.anr), c "{v[132:135]}"(s.anr2), c "{v[136:143]}"(s.x5)
#define P1_W(c, s) c "{v[180:211]}"(s.w0), c "{v[212:219]}"(s.w1)
#define P1_S [sx] "+&s"(sx), [sw] "+&s"(sw), [sb] "+&s"(sb)
#define P1_IN(s) "{v[220:235]}"(s.a0), P1_IN2(s)
#define P1_IN2(s) "{v[236:238]}"(s.a1), [rx] "s"(rx), [ra] "s"(ra), [rb] "s"(rb), [m2] "s"(lds0 + w * 2048), [m1] "s"(lds0 + w * 1024), \
    [m0q] "s"(lds0 + w * 256), [e2] "s"(lds0 + (w & 3)), [l0] "s"(lds0), [rw] "s"(rw)
DI u32x4 srd(const char* p, unsigned n) { const unsigned long long a = (unsigned long long)p; return (u32x4){(unsigned)a, (unsigned)(a >> 32) & 0xffffu, n, 0x00020000u}; }
#define ACC(m, tt) (f32x4){st.acc[8 * (m) + 4 * (tt)], st.acc[8 * (m) + 4 * (tt) + 1], st.acc[8 * (m) + 4 * (tt) + 2], st.acc[8 * (m) + 4 * (tt) + 3]}
#define ACN(m, tt) (f32x4){st.an[8 * (m) + 4 * (tt)], st.an[8 * (m) + 4 * (tt) + 1], st.an[8 * (m) + 4 * (tt) + 2], st.an[8 * (m) + 4 * (tt) + 3]}

template <bool TT> DI void phase1(const Params& P, ldsp lds, int i, int j, P1State& st, u32x4 rx, u32x4 ra, u32x4 rb, unsigned sx, unsigned sw, unsigned sb, unsigned long long& tag) {
  const int w = __builtin_amdgcn_readfirstlane(threadIdx.x >> 6), rg = w & 3, ch = w >> 2;
  const rsrc_t ws = __builtin_amdgcn_make_buffer_rsrc((void*)P.ws, 0, WS_END, 0x00020000);
  const unsigned lds0 = (unsigned)(size_t)lds;
  const int jm = j & 7;
  const u32x4 rw = srd(P.ws, WS_END);
  if constexpr (TT) {
    const unsigned l = threadIdx.x & 63, g0 = (l & 15) * 16, g1 = (jm * 16 + rg * 4 + (l >> 4)) * XROW + g0;
    asm volatile(P1A_T : P1_X("=&", st), P1_ACC("=&", st), P1_ACN("=&", st), P1_W("=&", st), "=&{v239}"(st.gt), P1_S
                 : P1_IN(st), [g0] "v"(g0), [g1] "v"(g1), [g2] "v"(l & 8 ? 0x7fff0000u : g1), [rgw] "s"(srd(P.gw, XROW)), [ch] "s"(ch) : "scc", "memory"); }
  else asm volatile(P1A_F : P1_X("=", st), P1_ACC("=", st), P1_ACN("=", st), P1_W("=", st), P1_S : P1_IN(st), [ch] "s"(ch) : "scc", "memory");
  int lane = fresh(threadIdx.x) & 63, u = lane & 15, g = lane >> 4;
  const int vrow = (u >> 1) + 8 * (u & 1);
  if constexpr (TT) {
    const bool isq = jm < 4;
    if (isq) { st.acc *= QSCALE; st.accr *= QSCALE; }
    const int hs = isq ? Q_HEAD : KV_HEAD, base = (isq ? WS_Q + i * Q_GRP : WS_KV + i * KV_GRP) + (jm & 3) * 8 * hs, vo = (g >> 1) * hs + (g & 1) * 512 + u * 16;
#pragma unroll
    for (int pp = 0; pp < 2; ++pp)
#pragma unroll
      for (int tt = 0; tt < 2; ++tt) st128s(ws, vo, base + (4 * ch + 2 * pp) * hs + rg * 1024 + (tt ^ ch) * 256, pk8(ACC(2 * pp, tt), ACC(2 * pp + 1, tt)));
#pragma unroll
    for (int e = 0; e < 2; ++e) __builtin_amdgcn_raw_buffer_store_b32(pk2(st.accr[2 * e], st.accr[2 * e + 1]), ws, 2 * g * hs + u * 4, base + e * hs + 4096 + rg * 128 + ch * 64, AUX);
  } else {
    const int base = WS_KV + i * KV_GRP + 4 * jm * KV_HEAD + 4608 + (4 * rg + g) * 320;
#pragma unroll
    for (int e = 0; e < 2; ++e) st128(ws, base + (2 * ch + e) * KV_HEAD + vrow * 16, pk8s(ACN(e, 0), ACN(e, 1), ch));
    if (ch) {
      const int od = u >> 3, rr = (u + 2 * od) & 3; u32x4 v = pk8(st.anr2, st.anr);
      if (rr >= 2) v = rr == 2 ? (u32x4){0x3f803f80u, 0x3f803f80u, 0x3f803f80u, 0x3f803f80u} : u32x4{};
      st128(ws, base + (((u >> 1) & 2) + od) * KV_HEAD + (16 + rr) * 16, v); }
  }
  if (ch) __builtin_amdgcn_s_setprio(1);
  int h2, c2; blk2(h2, c2);
  tag = get_tag(lds);
  const int fa = F_A + (h2 * 8 + c2) * 384 + (lane < 34 && (lane & 15) < 5 ? lane : 0) * 8;
  unsigned done = w == 7 ? 2 : 0; u32x2 pk;
#define P1F_IN [l16] "v"(lane * 16), [sq] "s"(WS_Q + h2 * Q_HEAD + 2 * c2 * Q_GRP + w * 1024), [sk] "s"(WS_KV + h2 * KV_HEAD + w * 1024), \
    [m7] "s"(lds0 + (w < 2 ? LDS_Q + 7168 : 40960) + w * 1024), [s7] "s"((w < 2 ? WS_Q + h2 * Q_HEAD + 2 * c2 * Q_GRP + 7168 : WS_KV + h2 * KV_HEAD + 40960) + w * 1024)
  { const u32x2 tv = {(unsigned)tag, (unsigned)(tag >> 32)};
    const char* fl = P.ws + F_A;
    const int fo = TT ? (jm < 4 ? ((8 * jm + (lane & 7)) * 8 + (i >> 1)) * 384 + (32 + (i & 1)) * 8 : (8 * (jm & 3) * 8 + lane) * 384 + i * 8)
                      : (4 * jm * 8 + (lane & 31)) * 384 + (16 + i) * 8;
#define P1B_OPS P1_W("+", st), P1_S, [pk] "=&v"(pk), [done] "+&s"(done) \
      : P1_X("", st), P1_IN(st), [vz] "v"(fo), [tag] "v"(tv), [fl] "s"(fl), [fa] "v"(fa - F_A), P1F_IN : "scc", "vcc", "memory"
    if constexpr (TT) asm volatile(P1B_T : P1_ACC("=&", st), P1_ACN("+", st), P1B_OPS); else asm volatile(P1B_F : P1_ACC("+", st), P1B_OPS); }
  u = fresh(lane) & 15; g = fresh(lane) >> 4;
  auto fetch = [&]() {
    if (!done) { done = 1; poll(P, fa, tag); asm volatile(P1FETCH : [sw] "=&s"(sw) : [m1] "s"(lds0 + w * 1024), [l0] "s"(lds0), [rw] "s"(rw), P1F_IN : "scc", "memory"); }
  };
  if constexpr (TT) {
    fetch();
    const int base = WS_PQ + i * Q_GRP + 4 * jm * Q_HEAD, vo = (g >> 1) * Q_HEAD + (g & 1) * 512 + u * 16;
#pragma unroll
    for (int tt = 0; tt < 2; ++tt) { const u32x2 a = phi4(ACC(0, tt)), b = phi4(ACC(1, tt));
      st128s(ws, vo, base + 2 * ch * Q_HEAD + rg * 1024 + (tt ^ ch) * 256, (u32x4){a[0], a[1], b[0], b[1]}); }
    __builtin_amdgcn_raw_buffer_store_b32(phi2(g < 2 ? st.accr[0] : st.accr[2], g < 2 ? st.accr[1] : st.accr[3]), ws, (2 * (g & 1) + (g >> 1)) * Q_HEAD + u * 4, base + 4096 + rg * 128 + ch * 64, AUX);
    if (ch) { float a = __uint_as_float(st.gt);
#define ROR(n) a += __int_as_float(__builtin_amdgcn_update_dpp(0, __float_as_int(a), 0x120 + n, 0xf, 0xf, false))
      ROR(8); ROR(4); ROR(2); ROR(1);
      const float gt = __builtin_amdgcn_rcpf(1.f + __builtin_amdgcn_exp2f(-a * LOG2E));
      st32(ws, u < 2 ? WS_G + u * 8192 + (i * 128 + jm * 16 + rg * 4 + g) * 4 : WS_END, __float_as_uint(u ? 1.f - gt : gt)); }
  } else {
    { ldsp f0 = (ldsp)(size_t)fresh(lds0 + LDS_SC + rg * 9216 + ch * 2056 + lane * 16), f1 = (ldsp)(size_t)fresh((int)(size_t)f0 ^ 8);
#pragma unroll
      for (int tt = 0; tt < 2; ++tt) { ldsp f = tt ? f1 : f0;
        *(LAS u32x2*)(f + 4096) = pk4(ACC(2, tt)); *(LAS u32x2*)(f + 5120) = pk4(ACC(3, tt)); *(LAS u32x2*)f = phi4(ACC(0, tt)); *(LAS u32x2*)(f + 1024) = phi4(ACC(1, tt)); }
      *(LAS u32x2*)(f0 + 8192 - ch * 2048) = phi4(st.accr);
      *(LAS u32x2*)(f0 + (LDS_SC9 - LDS_SC - rg * 8192 - ch * 2048)) = u == 0 ? (u32x2){0x3f803f80u, 0x3f803f80u} : pk4(st.accr); }
    fetch();
  }
}
struct KvFin { unsigned pa, pb[4], om, orr; u32x4 a[8], b[8]; };
DI void kv_addr(KvFin& k, unsigned lds0, int hh, int lane) {
  const int u = lane & 15, up = (lane >> 4) & 1, h2 = lane >> 5, lo = (u + 16 * h2) * 16;
  k.pa = lds0 + LDS_SC + (up ? 8 : hh) * 1024 + lo;
#pragma unroll
  for (int r = 0; r < 4; ++r) k.pb[r] = lds0 + (up ? LDS_SC9 + r * 1024 : LDS_SC + (4 + hh) * 1024 + r * 9216) + lo;
  const int e = up ? (u < 6 ? 18 + u : u - 2 * hh + 8) : (u >> 1) + 8 * (u & 1);
  const bool sel = !up || (u >> 1) == 4 + hh;
  k.om = sel || u < 2 ? e * 32 + h2 * 16 : 0x7fff0000; k.orr = (sel || u < 6) && h2 == (hh >> 1) ? 1280 + e * 8 : 0x7fff0000;
  asm volatile("" : "+v"(k.pa), "+v"(k.pb[0]), "+v"(k.pb[1]), "+v"(k.pb[2]), "+v"(k.pb[3]), "+v"(k.om), "+v"(k.orr));
}
DI void kv_read(KvFin& k) {
#pragma unroll
  for (int r = 0; r < 8; ++r) { k.a[r] = lds128((ldsp)(size_t)k.pa + (r >> 1) * 9216 + (r & 1) * 512); k.b[r] = lds128((ldsp)(size_t)k.pb[r >> 1] + (r & 1) * 512); }
}
DI void kv_finish(rsrc_t ws, const KvFin& k, int fidx, int hh) {
  const int base = WS_KVP + ((4 * ((fidx >> 4) & 7) + hh) * 16 + (fidx & 15)) * KVP_SZ;
  f32x16 c = {};
#pragma unroll
  for (int r = 0; r < 8; ++r) { c = mfma32(k.a[r], k.b[r], c); SB0(); __builtin_amdgcn_s_sleep(1); SB0(); }
  st128s(ws, k.om, base, __builtin_bit_cast(u32x4, (f32x4){c[0], c[1], c[2], c[3]}));
  st128s(ws, k.om, base + 640, __builtin_bit_cast(u32x4, (f32x4){c[4], c[5], c[6], c[7]}));
  if (hh & 1) __builtin_amdgcn_raw_buffer_store_b64(__builtin_bit_cast(u32x2, (f32x2){c[10], c[11]}), ws, k.orr, base, AUX);
  else __builtin_amdgcn_raw_buffer_store_b64(__builtin_bit_cast(u32x2, (f32x2){c[8], c[9]}), ws, k.orr, base, AUX);
}
DI unsigned geo_bm(int lane) { return ((lane >> 5) * 20 + ((lane >> 4) & 1) * 8 + 2 * ((lane >> 2) & 3)) * 144 + 8 * (lane & 3); }
DI unsigned geo_br(int lane) { return (72 * (lane >> 4) + 16 + ((lane >> 2) & 3)) * 144 + 8 * (lane & 3); }
DI int geo_mo(int tg, int tq, int lane) { return (tg >> 1) * Q_GRP + (2 * (tg & 1) + (tq >> 1)) * 1024 + ((lane >> 4) & 1) * 512 + (16 * (tq & 1) + (lane & 15)) * 16 + (lane >> 5) * Q_HEAD; }
DI int geo_ro(int tg, int tq, int lane) { return (tg >> 1) * Q_GRP + 4096 + (2 * (tg & 1) + (tq >> 1)) * 128 + (16 * (tq & 1) + (lane & 15)) * 4 + 4 * (lane >> 4) * Q_HEAD; }
DI void phase1_entry(const Params& P, ldsp lds, unsigned long long& tag, int& fidx, unsigned& v0) {
  const int b = blockIdx.x, x = b & 7, r = b >> 3;
  const int tid = threadIdx.x, lane = tid & 63, w = __builtin_amdgcn_readfirstlane(tid >> 6), rg = w & 3, ch = w >> 2;
  const unsigned lds0 = (unsigned)(size_t)lds;
  if (w == 3) {
    unsigned long long pc; unsigned len;
    asm volatile("s_getpc_b64 %0\n.Lgm_first:\n\ts_mov_b32 %1, .Lgm_last-.Lgm_first+512" : "=s"(pc), "=s"(len));
    const unsigned o = (lane * 32 + r) * 128;
    touch((const char*)pc, o < len ? o : 0, lds0 + LDS_DUMMY);
  }
  const int i = (x >> 2) * 8 + (r & 7), j = (x & 3) * 2 + ((r >> 3) & 1) + 8 * (r >> 4), jm = j & 7;
  const bool tt = j < 8;
  fidx = j * 16 + i;
  P1State st;
  const int row = 16 * w + (lane >> 3);
  st.a0[12] = row * XROW + (((lane & 7) ^ ((row >> 1) & 7)) << 4); st.a0[13] = (row + 8) * XROW + (((lane & 7) ^ (((row + 8) >> 1) & 7)) << 4);
  const unsigned sx0 = ((i + 2 * j) % 9) * 128;
  unsigned sx = sx0, sw = sx0 * 1728, sb = sw;
  const u32x4 rx = srd(P.x + (size_t)i * 128 * XROW, 128 * XROW), ra = srd(P.wa, 576 * WROW), rb = srd(P.wb, 576 * WROW);
  asm volatile("s_mov_b32 m0, %4\n\ts_nop 0\n\tbuffer_load_dwordx4 %0, %2, %3 offen lds\n\ts_add_u32 m0, %4, 1024\n\ts_nop 0\n\tbuffer_load_dwordx4 %1, %2, %3 offen lds"
               :: "v"(st.a0[12]), "v"(st.a0[13]), "s"(rx), "s"(sx), "s"(lds0 + w * 2048) : "scc", "memory");
  { const int cw = tt ? 288 * jm : 1152 + 144 * jm, sp = tt ? 0 : 1008, cn = tt ? 144 * jm : 2304 + 144 * jm;
    st.a0[14] = woff<true>(0, w, lane, cw, sp); st.a0[15] = woff<true>(1, w, lane, cw, sp); st.a1[0] = woff<true>(2, w, lane, cw, sp);
    st.a1[1] = woff<false>(0, w, lane, cn, 0); st.a1[2] = woff<false>(1, w, lane, cn, 0); }
  { const u32x4 rw = ra; asm volatile(P1P0 : P1_S : P1_IN(st), [tt] "s"(j) : "scc", "memory"); }
  if (w == 4) {
    int h2, c2; blk2(h2, c2); const int me = h2 * 8 + c2, f = lane & 7;
    touch_sc1(P.ws, lane & 8 ? F_A + me * 384 + (f < 3 ? f : 0) * 128 : F_OB + ((f >> 1) & 1) * 65536 + me * 256 + (f & 1) * 128, lds0 + LDS_DUMMY); }
  if (tid < 26) *(LAS unsigned*)(lds + LDS_SYNC + tid * 4) = 0u;
  if (w == 7) asm volatile("s_mov_b32 m0, %2\n\ts_nop 0\n\tglobal_load_lds_dword %0, %1 sc1" :: "v"((lane & 1) * 4), "s"(P.ws + F_EP), "s"(lds0 + LDS_EP) : "memory");
  {
    const int u = lane & 15, g = lane >> 4, q = u >> 2, p = u & 3;
    const int rowW = (4 * g + q) * 288, rowN = ((g >> 1) + 2 * (4 * (g & 1) + q)) * 144;
    const int pat = tt ? (p & 1) * 16 + (p >> 1) * 40 : 8 * p;
    const unsigned wb0 = lds0 + LDS_W;
    st.a0[4] = wb0 + rowW + pat + (tt ? 144 : 72) * ch; st.a0[6] = wb0 + rowW + 72 * p + 32;
    st.a0[8] = wb0 + rowN + pat + 72 * ch; st.a0[10] = wb0 + rowN + 72 * (p & 1) + 32;
#pragma unroll
    for (int e = 5; e < 12; e += 2) st.a0[e] = st.a0[e - 1] + 55296;
    const int xr0 = (rg * 32 + ch * 16 + u) * 128 + ((g ^ (u >> 1)) << 4);
    st.a0[0] = lds0 + xr0; st.a0[1] = lds0 + (xr0 ^ 2048); st.a0[2] = lds0 + (xr0 ^ 64); st.a0[3] = lds0 + (xr0 ^ 64 ^ 2048); }
  { int h2, c2; blk2(h2, c2);
    const int r32 = lane & 31, hh = lane >> 5, un = rg * 64 + lane;
    v0 = (un / 9) * XROW + (un % 9) * 16 + 144 * c2;
    if (w == 0) { const int pos = 16 * ((r32 >> 2) & 1) + 4 * (r32 >> 3) + (r32 & 3);
      *(LAS u32x4*)(lds + LDS_T0 + lane * 16) = (u32x4){0xddddddddu, lds0 + hh * 512 + pos * 16, lds0 + 4096 + pos * 4, r32 < 19 ? lds0 + 4608 + hh * 320 + r32 * 16 : 0x100000u}; }
    if (w == 1) *(LAS u32x4*)(lds + LDS_T1 + lane * 16) = (u32x4){lds0 + geo_bm(lane), lds0 + geo_br(lane), (unsigned)geo_mo(h2, 0, lane), (unsigned)geo_ro(h2, 0, lane)};
    if (w == 2) { const int u1 = 256 + r32, e32 = r32 < 19 ? r32 : 18;
      *(LAS u32x4*)(lds + LDS_T2 + lane * 16) = (u32x4){(unsigned)((u1 / 9) * XROW + (u1 % 9) * 16 + 144 * c2), lds0 + LDS_T0 + hh * 320 + e32 * 16, hh ? 0x100000u : lds0 + LDS_T0 + 640 + e32 * 4,
          (unsigned)(hh ? F_ALL + fidx * 8 : F_B + (tt ? (lane * 8 + (i >> 1)) * 256 + (16 + (i & 1) * 8 + j) * 8 : (32 * jm + lane) * 256 + i * 8))}; } }
  asm volatile("" : "+{v[220:235]}"(st.a0), "+{v[236:238]}"(st.a1), "+v"(v0));
  if (tt) phase1<true>(P, lds, i, j, st, rx, ra, rb, sx, sw, sb, tag); else phase1<false>(P, lds, i, j, st, rx, ra, rb, sx, sw, sb, tag);
}
struct AttState { f32x32 X, Y; f32x16 O0, O1; u32x4 Q0, Q1, QR, AD; u32x8 KB; u32x16 VB; u32x8 KR; u32x4 PG, GV; unsigned A2, A3; u32x2 SA; };
#define ATT_OPS(s) "+{v[0:31]}"(s.X), "+{v[32:63]}"(s.Y), "+{v[64:79]}"(s.O0), "+{v[80:95]}"(s.O1), "+{v[108:111]}"(s.AD), "+{v[112:119]}"(s.KB), "+{v[120:135]}"(s.VB), "+{v[136:143]}"(s.KR), "+{v[144:147]}"(s.PG), "={v[148:151]}"(s.GV), "+{v152}"(s.A2), "+{v156}"(s.A3)
#define ATT_INS(s) "{v[96:99]}"(s.Q0), "{v[100:103]}"(s.Q1), "{v[104:107]}"(s.QR)
DI int att_loop(AttState& s, u32x4 wsd, int om, int orr) {
  int n = 4, t, sp, bad; unsigned long long m;
  asm volatile(ATT_LOOP : ATT_OPS(s), [n] "+&s"(n), [t] "=&s"(t), [sp] "=&s"(sp), [bad] "=&s"(bad), [m] "=&s"(m)
               : ATT_INS(s), "{v[154:155]}"(s.SA), [inc] "s"(4 * KV_GRP), [ws] "s"(wsd), [om] "s"(om), [orr] "s"(orr), [lo] "s"(0x1p-30f), [hi] "s"(0x1p60f)
               : "v153", "scc", "vcc", "memory");
  return bad;
}
DI float xhalf0(float v) {
  auto r = __builtin_amdgcn_permlane32_swap(__float_as_uint(v), __float_as_uint(v), false, false); return __uint_as_float(r[0]); }
DI float xmax(float v) {
  auto r = __builtin_amdgcn_permlane32_swap(__float_as_uint(v), __float_as_uint(v), false, false); return fmaxf(__uint_as_float(r[0]), __uint_as_float(r[1])); }
DI void store_o(rsrc_t ws, int base, int tile, int lane, const f32x16& o, float rl) {
  st128(ws, base + tile * 1024 + lane * 16, (u32x4){pk2(o[0] * rl, o[1] * rl), pk2(o[2] * rl, o[3] * rl), pk2(o[4] * rl, o[5] * rl), pk2(o[6] * rl, o[7] * rl)});
  if (lane < 32) st32(ws, base + 4096 + tile * 128 + lane * 4, pk2(o[8] * rl, o[9] * rl));
}
struct HReg { unsigned one, sa, su; u32x2 tv; };
template <int VM, int N> DI void h_arrive(const HReg& R) {
  asm volatile("s_waitcnt vmcnt(%2) lgkmcnt(0)\n\tds_add_u32 %0, %1 offset:%3" :: "v"(R.sa), "v"(R.one), "i"(VM), "i"(N * 4) : "memory"); }
template <int SL, int N> DI void h_await(const HReg& R) {
#pragma nounroll
  for (int spin = 0; spin < (1 << 22); ++spin) {
    unsigned v, c; asm volatile("ds_read_b32 %0, %2 offset:%3\n\ts_waitcnt lgkmcnt(0)\n\ts_setprio 3\n\tv_readfirstlane_b32 %1, %0\n\ts_setprio 0" : "=&v"(v), "=s"(c) : "v"(R.su), "i"(N * 4) : "memory");
    if (c >= 4u) break;
    __builtin_amdgcn_s_sleep(SL); }
}
template <int SL> DI void h_poll(u32x4 wsd, unsigned vo, int so, unsigned long long tag) {
#pragma nounroll
  for (int spin = 0; spin < (1 << 20); ++spin) {
    u32x2 v; unsigned long long m;
    asm volatile("buffer_load_dwordx2 %0, %2, %3, %4 offen sc1\n\ts_waitcnt vmcnt(0)\n\ts_setprio 3\n\tv_cmp_ne_u64 %1, %5, %0\n\ts_setprio 0" : "=&v"(v), "=s"(m) : "v"(vo), "s"(wsd), "s"(so), "s"(tag) : "memory");
    if (!m) break;
    __builtin_amdgcn_s_sleep(SL); }
}
#define HI() do { SB0(); __builtin_amdgcn_s_setprio(3); SB0(); } while (0)
#define LO() do { SB0(); __builtin_amdgcn_s_setprio(0); SB0(); } while (0)
DI void lds_await(ldsp lds, int n) {
#pragma nounroll
  for (int spin = 0; spin < (1 << 22); ++spin) {
    if (__builtin_amdgcn_readfirstlane(__hip_atomic_load((LAS unsigned*)(lds + LDS_SYNC + n * 4), __ATOMIC_RELAXED, __HIP_MEMORY_SCOPE_WORKGROUP)) >= 4u) break;
    __builtin_amdgcn_s_sleep(2); }
  asm volatile("" ::: "memory");
}
constexpr int GDONE = 7;
constexpr int WHALF = 288 * 144;
DI void wo_half(const char* wo, int hf, int tq, unsigned v0, unsigned v1, unsigned dst) {
  wo += 288 * hf * XROW;
#pragma unroll
  for (int s = 0; s < 9; ++s) dma(wo + 32 * s * XROW, v0, dst + s * 4608 + tq * 1024);
  dma32(wo + 32 * tq * XROW, v1, dst + tq * 4608 + 4096); dma32(wo + 32 * (tq + 4) * XROW, v1, dst + (tq + 4) * 4608 + 4096);
  if (sfresh(tq) == 0) dma32(wo + 256 * XROW, v1, dst + 8 * 4608 + 4096);
}
constexpr int STASH = 2 * WHALF, XB = STASH + 4 * 9216;
static_assert(XB <= 13 * KV_GRP, "LDS map");
struct P3State { u32x32 w0, w1, w2, w3, w4; u32x16 w5; u32x4 w6; u32x32 o0; u32x4 o1; f32x16 a0; f32x4 a1; };
#define P3_W(c, s) c "{v[0:31]}"(s.w0), c "{v[32:63]}"(s.w1), c "{v[64:95]}"(s.w2), c "{v[96:127]}"(s.w3), c "{v[128:159]}"(s.w4), c "{v[160:175]}"(s.w5), c "{v[176:179]}"(s.w6)
#define P3_O(c, s) c "{v[180:211]}"(s.o0), c "{v[212:215]}"(s.o1)
#define P3_A(c, s) c "{v[216:231]}"(s.a0), c "{v[232:235]}"(s.a1)
DI f32x16 att_exact(rsrc_t ws, int h, u32x4 q, unsigned qq, int lane) {
      f32x16 o;
      const int kvo = WS_KV + h * KV_HEAD, r = lane & 31, hh = lane >> 5, pos = 16 * ((r >> 2) & 1) + 4 * (r >> 3) + (r & 3);
      const u32x4 qrem = {hh ? 0u : qq, 0u, 0u, 0u};
      float m = -INFINITY;
#pragma unroll
      for (int n = 0; n < 16; ++n) o[n] = 0.f;
#pragma nounroll
      for (int kt = 0; kt < 64; ++kt) {
        const int gb = kvo + (kt >> 2) * KV_GRP, t = kt & 3;
        const u32x4 km = ld128(ws, hh * 512 + pos * 16, gb + t * 1024);
        const unsigned kr = ld32(ws, 4096 + pos * 4, gb + t * 128);
        const int vo = hh * 320 + (r < 19 ? r : 19) * 16;
        const u32x4 v0 = ld128(ws, vo, gb + 4608 + t * 1280), v1 = ld128(ws, vo + 640, gb + 4608 + t * 1280);
        f32x16 sc = mfma32((u32x4){hh ? 0u : kr, 0u, 0u, 0u}, qrem, f32x16{});
        sc = mfma32(km, q, sc);
        float mx = sc[0];
#pragma unroll
        for (int n = 1; n < 16; ++n) mx = fmaxf(mx, sc[n]);
        const float mn = fmaxf(m, xmax(mx)), al = __builtin_amdgcn_exp2f(m - mn); m = mn;
        u32x8 p0, p1;
#pragma unroll
        for (int n = 0; n < 8; ++n) { p0[n] = __float_as_uint(__builtin_amdgcn_exp2f(sc[n] - mn)); p1[n] = __float_as_uint(__builtin_amdgcn_exp2f(sc[8 + n] - mn)); }
#pragma unroll
        for (int n = 0; n < 16; ++n) o[n] *= al;
        typedef __attribute__((ext_vector_type(16))) __bf16 bf16x16;
        o = __builtin_amdgcn_smfmac_f32_32x32x32_bf16(__builtin_bit_cast(bf16x8, v0), __builtin_bit_cast(bf16x16, p0), o, (int)0xdddddddd, 0, 0);
        o = __builtin_amdgcn_smfmac_f32_32x32x32_bf16(__builtin_bit_cast(bf16x8, v1), __builtin_bit_cast(bf16x16, p1), o, (int)0xdddddddd, 0, 0);
      }
      return o;
}

DI void phase23(const Params& P, ldsp lds, unsigned long long tag, int fidx, unsigned v0) {
  int h, c; blk2(h, c); h = __builtin_amdgcn_readfirstlane(h); c = __builtin_amdgcn_readfirstlane(c);
  const int tid = fresh(threadIdx.x), lane = tid & 63, w = __builtin_amdgcn_readfirstlane(tid >> 6), qg = w & 3, kh = w >> 2;
  const rsrc_t ws = __builtin_amdgcn_make_buffer_rsrc((void*)P.ws, 0, WS_END, 0x00020000);
  const unsigned lds0 = (unsigned)(size_t)lds;
  const int qoff = h * Q_HEAD + (2 * c + (qg >> 1)) * Q_GRP, t0 = 2 * (qg & 1);
  const int tg = h, cg = c, tq = qg;
  const u32x4 wsd = srd(P.ws, WS_END);
  P3State z;
  unsigned ts, mo, ro, bm, br;
  auto tables = [&](int t) { ldsp p = lds + fresh(lane * 16); const u32x4 r = lds128(p + t), t1 = lds128(p + LDS_T1);
    bm = t1[0]; br = t1[1]; mo = t1[2] + (tq >> 1) * 1024 + (tq & 1) * 256; ro = t1[3] + tq * 64; return r; };
  if (kh == 0) {
    AttState s;
    s.AD = tables(LDS_T0);
    s.PG = (u32x4){lane == 0 ? lds0 + LDS_SYNC + (GDONE + 1) * 4 : 0x100000u, 1u, lds0 + LDS_SYNC, lds0 + LDS_GW + (qg * 64 + (lane & 31)) * 4};
    s.SA = (u32x2){lane * 16u, lane >= 32 ? 0x7ffffff0u : lane * 4u};
    unsigned xa = lds0 + XB + tq * 5120 + lane * 16, su = lds0 + LDS_SYNC;
    asm volatile("" : "+{v240}"(xa), "+{v241}"(su), "+{v236}"(bm), "+{v237}"(br), "+{v238}"(mo), "+{v239}"(ro), "+{v[154:155]}"(s.SA));
    { const unsigned qp = lds0 + LDS_Q + (qg >> 1) * Q_GRP;
      asm volatile(ATT_PRO : "=&{v[0:31]}"(s.X), "=&{v[32:63]}"(s.Y), "=&{v[64:79]}"(s.O0), "=&{v[80:95]}"(s.O1), "=&{v[112:119]}"(s.KB), "=&{v[120:135]}"(s.VB), "=&{v[136:143]}"(s.KR),
                     "=&{v[96:99]}"(s.Q0), "=&{v[100:103]}"(s.Q1), "=&{v[104:107]}"(s.QR), "+{v[144:147]}"(s.PG), "=&{v152}"(s.A2), "=&{v156}"(s.A3)
                   : "{v[108:111]}"(s.AD), [qa] "v"(qp + t0 * 1024 + lane * 16), [qr] "v"(qp + 4096 + t0 * 128 + lane * 4), [f] "s"(fidx) : "scc", "memory"); }
    __builtin_amdgcn_s_setprio(2);
    const int bad = att_loop(s, wsd, WS_OA + qoff + t0 * 1024, WS_OA + qoff + 4096 + t0 * 128);
    if (__builtin_expect(bad, 0)) {
#pragma nounroll
      for (int e = 0; e < 2; ++e) if (bad >> e & 1) {
        const int l2 = fresh(lane);
        const f32x16 o = att_exact(ws, h, e ? s.Q1 : s.Q0, __shfl(s.QR[0], (l2 & 31) + 32 * e), l2);
        store_o(ws, WS_OA + qoff, t0 + e, l2, o, __uint_as_float(e ? s.GV[3] : s.GV[2]) * __builtin_amdgcn_rcpf(xhalf0(o[10]))); }
    }
    __builtin_amdgcn_s_setprio(0);
    if (__builtin_amdgcn_readfirstlane(s.GV[1]) < 4u) lds_await(lds, 7);
    { unsigned tx, sp;
      asm volatile(P3_RDA : P3_W("=&", z), P3_A("=&", z), P3_O("=&", z), [t] "=&s"(ts), [x] "=&v"(tx), [sp] "=&s"(sp)
                   : [bm] "v"(bm), [br] "v"(br), [mo] "v"(mo), [ro] "v"(ro), [xa] "v"(xa), [su] "v"(su), [ws] "s"(wsd), [so] "s"(WS_OA) : "scc", "memory"); }
  } else {
    constexpr unsigned OOB = 0x7ffffff0u;
    const int kb = WS_KVP + h * (16 * KVP_SZ);
    const u32x4 t2 = tables(LDS_T2);
    HReg R;
    R.one = 1u; R.sa = lane == 0 ? lds0 + LDS_SYNC : 0x100000u; R.su = lds0 + LDS_SYNC; R.tv = (u32x2){(unsigned)tag, (unsigned)(tag >> 32)};
    unsigned l16 = lane * 16, l8 = lane * 8, l4 = lane * 4, l4z = lane >= 32 ? OOB : lane * 4, l4d = (lane & 31) * 4, tch = (lane < 41 - 2 * (qg == 3) ? lane : 0) * 128;
    unsigned pfo = l4d * 2, fob = lane < 8 ? lane * 256 : OOB, k8 = lane < 46 ? l8 : OOB;
    unsigned ktw = lane < 46 ? lds0 + LDS_T0 + qg * 184 + lane * 4 : 0x100000u, v1 = t2[0], ktr = t2[1], ktr2 = t2[2], fbo = t2[3];
    bm += (tq & 1) * WHALF; br += (tq & 1) * WHALF;
    u32x4 ones = {~0u, ~0u, ~0u, ~0u};
    u32x2 a2 = {0u, 0u}, p2[2] = {{0u, 0u}, {0u, 0u}};
    asm volatile("" : "+v"(R.one), "+v"(R.sa), "+v"(R.su), "+v"(R.tv), "+v"(l16), "+v"(l8), "+v"(l4), "+v"(l4z), "+v"(l4d), "+v"(tch), "+v"(fbo), "+v"(pfo), "+v"(fob), "+v"(k8));
    asm volatile("" : "+v"(ktw), "+v"(ktr), "+v"(ktr2), "+v"(v0), "+v"(v1), "+v"(bm), "+v"(br), "+v"(mo), "+v"(ro), "+v"(a2), "+v"(p2[0]), "+v"(p2[1]), "+v"(ones));
    KvFin kf;
    if (fidx >= 128) kv_addr(kf, lds0, qg, lane);
    const u32x2 seen = __builtin_amdgcn_raw_buffer_load_b64(ws, pfo, F_A + (h * 8 + c) * 384, AUX);
    if (fidx >= 128) wait_bar<1>(); else wait_bar<5>();
    __builtin_amdgcn_s_setprio(3);
    const bool all = __all(__builtin_bit_cast(unsigned long long, seen) == tag);
    LO();
    if (sfresh(fidx) >= 128) { kv_read(kf); h_arrive<63, 5>(R); h_await<1, 5>(R); }
    if (!all) h_poll<4>(wsd, pfo, F_A + (h * 8 + c) * 384, tag);
    const char* kvb = P.ws + WS_KV + h * KV_HEAD + (47 + qg) * 1024;
#pragma unroll
    for (int n = 0; n < 9; ++n) dma_sc1(kvb + n * 4096, l16, lds0 + (47 + qg) * 1024 + n * 4096);
    if (sfresh(fidx) >= 128) { HI(); kv_finish(ws, kf, fidx, qg); LO(); }
#pragma unroll
    for (int e = 0; e < 2; ++e) {
      st128s(ws, l16, WS_OA + qoff + (t0 + e) * 1024, ones); __builtin_amdgcn_raw_buffer_store_b32(ones[0], ws, l4z, WS_OA + qoff + 4096 + (t0 + e) * 128, AUX); }
    h_arrive<0, 0>(R);
    h_await<4, GDONE + 1>(R);
    if (w == 4) { h_await<2, 0>(R); __builtin_amdgcn_raw_buffer_store_b64(R.tv, ws, fbo, 0, AUX); dma_sc1(kvb + 26 * 4096, l16, lds0 + 151 * 1024); }
#pragma unroll
    for (int n = 9; n < 26; ++n) dma_sc1(kvb + n * 4096, l16, lds0 + (47 + qg) * 1024 + n * 4096);
    h_arrive<0, 25>(R);
    { const int o = ((blockIdx.x >> 3) * 162 + qg * 41) * 128; touch(P.wob + o, tch, lds0 + LDS_DUMMY); touch(P.woa + o, tch, lds0 + LDS_DUMMY); }
    h_poll<12>(wsd, pfo, F_B + (h * 8 + c) * 256, tag);
    { f32x2 kp[16]; u32x4 pq[2]; float gg[2];
#pragma unroll
      for (int n = 0; n < 16; ++n) kp[n] = __builtin_bit_cast(f32x2, __builtin_amdgcn_raw_buffer_load_b64(ws, k8, kb + qg * 368 + n * KVP_SZ, AUX));
#pragma unroll
      for (int e = 0; e < 2; ++e) { pq[e] = ld128(ws, l16, WS_PQ + qoff + (t0 + e) * 1024); p2[e][0] = ld32(ws, l4z, WS_PQ + qoff + 4096 + (t0 + e) * 128);
        gg[e] = __uint_as_float(ld32(ws, l4d, WS_G + 8192 + (c * 256 + qg * 64 + e * 32) * 4)); }
      touch_sc1(P.ws + WS_G + (c * 256 + qg * 64) * 4, l4, lds0 + LDS_GW + qg * 256);
      asm volatile("s_waitcnt vmcnt(0)" ::: "memory");
      HI();
#pragma unroll
      for (int d = 1; d < 16; d *= 2)
#pragma unroll
        for (int n = 0; n < 16; n += 2 * d) kp[n] += kp[n + d];
      *(LAS unsigned*)(size_t)ktw = pk2(kp[0][0], kp[0][1]);
      LO();
      h_arrive<0, 4>(R); h_await<4, 4>(R);
      const u32x4 am = *(LAS u32x4*)(size_t)ktr; a2[0] = *(LAS unsigned*)(size_t)ktr2;
      asm volatile("s_waitcnt lgkmcnt(0)" ::: "memory");
      HI();
      f32x16 o[2];
#pragma unroll
      for (int e = 0; e < 2; ++e) { o[e] = mfma32(am, pq[e], f32x16{}); SB0(); __builtin_amdgcn_s_sleep(1); SB0(); }
#pragma unroll
      for (int e = 0; e < 2; ++e) { o[e] = __builtin_amdgcn_mfma_f32_32x32x8bf16_1k(__builtin_bit_cast(s16x4, a2), __builtin_bit_cast(s16x4, p2[e]), o[e], 0, 0, 0); SB0(); __builtin_amdgcn_s_sleep(1); SB0(); }
#pragma unroll
      for (int e = 0; e < 2; ++e) {
        const float rl = gg[e] * __builtin_amdgcn_rcpf(o[e][10] + 1e-6f); const f32x16& t = o[e];
        st128s(ws, l16, WS_OB + qoff + (t0 + e) * 1024, (u32x4){pk2(t[0] * rl, t[1] * rl), pk2(t[2] * rl, t[3] * rl), pk2(t[4] * rl, t[5] * rl), pk2(t[6] * rl, t[7] * rl)});
        __builtin_amdgcn_raw_buffer_store_b32(pk2(t[8] * rl, t[9] * rl), ws, l4z, WS_OB + qoff + 4096 + (t0 + e) * 128, AUX); } }
    unsigned stp = l16 + lds0 + STASH + tq * 9216, xa = l16 + lds0 + XB + (tq ^ 1) * 5120, s4 = l4 + lds0 + STASH + tq * 9216 + 8192, mo2 = mo ^ 256, ro2 = ro ^ 64;
    unsigned bm2 = bm + ((tq & 1) ^ 1) * WHALF, br2 = br + ((tq & 1) ^ 1) * WHALF, l0z = lane == 0 ? 0u : OOB;
    asm volatile("" : "+v"(xa), "+v"(bm2), "+v"(br2), "+v"(stp), "+v"(s4), "+v"(mo2), "+v"(ro2), "+v"(l0z));
    LO();
    h_await<16, GDONE + 5>(R); wo_half(P.wob, 0, tq, v0, v1, lds0);
    asm volatile("s_waitcnt vmcnt(0)" ::: "memory");
    __builtin_amdgcn_raw_buffer_store_b64(R.tv, ws, fob, F_OB + (c * 4 + qg) * 2048 + h * 8, AUX);
    h_await<16, GDONE + 9>(R); wo_half(P.wob, 1, tq, v0, v1, lds0 + WHALF);
    h_arrive<0, 1>(R); h_await<4, 1>(R);
    asm volatile(P3_RDP : P3_W("=&", z) : [bm] "v"(bm), [br] "v"(br) : "memory");
    h_arrive<63, 2>(R); h_await<4, 2>(R);
    wo_half(P.woa, 0, tq, v0, v1, lds0); wo_half(P.woa, 1, tq, v0, v1, lds0 + WHALF);
    h_poll<12>(wsd, pfo, F_OB + (tg * 8 + cg) * 256, tag);
    if (blockIdx.x == 0) h_poll<4>(wsd, l8, F_ALL + tq * 512, tag);
    h_arrive<0, 7>(R);
    const int sob = WS_OB + (tq & 1) * 16 * Q_HEAD;
    asm volatile(P3_LD : P3_O("=&", z), [t] "=&s"(ts) : [mo] "v"(mo2), [ro] "v"(ro2), [ws] "s"(wsd), [so] "s"(sob) : "scc", "memory");
    h_await<16, GDONE + 13>(R);
    asm volatile(P3_ST : [t] "=&s"(ts) : [mo] "v"(mo), [ro] "v"(ro), [sm] "s"(lds0 + STASH + tq * 9216), [ws] "s"(wsd), [so] "s"(sob) : "scc", "memory");
    h_await<2, 7>(R);
    if (blockIdx.x == 0 && w == 4) { __builtin_amdgcn_raw_buffer_store_b64(R.tv, ws, l0z, F_EP, AUX); asm volatile("s_waitcnt vmcnt(0)" ::: "memory"); }
    h_await<16, GDONE + 15>(R); h_await<4, GDONE + 16>(R);
    __builtin_amdgcn_s_setprio(1);
    asm volatile(P3_BB : P3_O("+", z), P3_A("=&", z), P3_W("+", z), [t] "=&s"(ts)
                 : [st] "v"(stp), [s4] "v"(s4), [xa] "v"(xa), [bm] "v"(bm2), [br] "v"(br2), [mo] "v"(mo), [ro] "v"(ro), [sa] "v"(R.sa), [one] "v"(R.one), [ws] "s"(wsd), [so] "s"(WS_OA + 16 * Q_HEAD) : "scc", "memory");
  }
  const int l3 = fresh(lane);
  unsigned xo = lds0 + STASH + tq * 9216 + l3 * 16, op = (tg * 64 + tq * 16 + (l3 & 15)) * XROW + cg * 144 + (l3 >> 4) * 8, op4 = l3 < 32 ? op + 128 : 0x7ffffff0u;
  { unsigned tx, sp; unsigned long long ret;
    asm volatile(P3_FIN : P3_W("+", z), P3_O("+", z), P3_A("+", z), [t] "=&s"(ts), [x] "=&v"(tx), [sp] "=&s"(sp), [ret] "=&s"(ret), "+v"(xo), "+v"(op), "+v"(op4)
                 : [mo] "v"(mo), [ro] "v"(ro), [ws] "s"(wsd), [so] "s"(WS_OA + 16 * kh * Q_HEAD) : "scc", "vcc", "memory"); }
  { const f32x4 acc[5] = {{z.a0[0], z.a0[1], z.a0[2], z.a0[3]}, {z.a0[4], z.a0[5], z.a0[6], z.a0[7]}, {z.a0[8], z.a0[9], z.a0[10], z.a0[11]}, {z.a0[12], z.a0[13], z.a0[14], z.a0[15]}, z.a1};
    const rsrc_t rso = __builtin_amdgcn_make_buffer_rsrc((void*)P.out, 0, 2048 * XROW, 0x00020000);
    auto give = [&](int m) { *(LAS f32x4*)((ldsp)(size_t)xo + m * 1024) = acc[m]; };
    auto take = [&](int m) { const f32x4 v = acc[m] + *(LAS f32x4*)((ldsp)(size_t)xo + m * 1024);
      __builtin_amdgcn_raw_buffer_store_b64((u32x2){pk2(v[0], v[1]), pk2(v[2], v[3])}, rso, m < 4 ? op + m * 32 : op4, 0, 0); };
    if (kh) { give(0); give(1); lds_bar(); take(2); take(3); take(4); }
    else { give(2); give(3); give(4); lds_bar(); take(0); take(1); }
  }
}

extern __shared__ __attribute__((aligned(16))) char smem[];
__global__ void __launch_bounds__(512, 2) gated_mix_kernel(Params P) {
  unsigned long long tag; int fidx; unsigned v0;
  phase1_entry(P, (ldsp)smem, tag, fidx, v0);
  phase23(P, (ldsp)smem, tag, fidx, v0);
}

extern "C" void kernel_launch(void* const* d_in, const int* in_sizes, int n_in, void* d_out, int out_size, void* d_ws, size_t ws_size, hipStream_t stream) {
  Params P;
  P.x = (const char*)d_in[0]; P.wa = (const char*)d_in[1]; P.woa = (const char*)d_in[2]; P.wb = (const char*)d_in[4]; P.wob = (const char*)d_in[5];
  P.gw = (const char*)d_in[6]; P.out = (char*)d_out; P.ws = (char*)d_ws;
  (void)hipFuncSetAttribute((const void*)gated_mix_kernel, hipFuncAttributeMaxDynamicSharedMemorySize, LDS_BYTES);
  gated_mix_kernel<<<256, 512, LDS_BYTES, stream>>>(P);
}
```
